```python
import torch

def _softmax(x, axis=-1):
    return torch.softmax(x, dim=axis)
def _gelu(x):
    return torch.nn.functional.gelu(x, approximate="tanh")
def _silu(x):
    return torch.nn.functional.silu(x)
def _sigmoid(x):
    return torch.sigmoid(x)
def _layernorm(x, axis=-1, eps=1e-5):
    m = x.mean(dim=axis, keepdim=True)
    v = x.var(dim=axis, keepdim=True, unbiased=False)
    return (x - m) / torch.sqrt(v + eps)
def _scatter_add(tgt, idx, src, axis=0):
    return tgt.clone().index_add_(axis, idx, src)
def _segment_sum(src, seg, n):
    out = torch.zeros((n,) + src.shape[1:], dtype=src.dtype)
    return out.index_add_(0, seg, src)
def _pad(x, pw):
    flat = tuple(v for pair in reversed(pw) for v in pair)
    return torch.nn.functional.pad(x, flat)

kernel_name = 'standalone__weight_only_int_quip__sampling'

def setup_inputs():

    _w = (lambda a: (lambda t: t.to(torch.bfloat16) if t.is_floating_point() else t)(torch.as_tensor(a)))
    B, S, D, F = 16, 4, 1024, 3584
    G = 32 if 32 > 0 else D
    D = (D // G) * G
    ng = D // G
    bits = 3 if 3 > 0 else 4
    qmax = 1 << bits
    qw = torch.randint(0, qmax, (D, F)).to(torch.int32)
    U, _ = (lambda *_a, **_k: torch.linalg.qr(*[torch.as_tensor(_x).float() for _x in _a], **_k))((lambda *s: torch.randn(*s, dtype=torch.bfloat16) * 0.05)(D, D))
    V, _ = (lambda *_a, **_k: torch.linalg.qr(*[torch.as_tensor(_x).float() for _x in _a], **_k))((lambda *s: torch.randn(*s, dtype=torch.bfloat16) * 0.05)(F, F))
    return {
        "x": _w((lambda *s: torch.randn(*s, dtype=torch.bfloat16) * 0.05)(B, S, D)),
        "qw": _w(qw),
        "scales": _w(torch.abs((lambda *s: torch.randn(*s, dtype=torch.bfloat16) * 0.05)(ng, F)) * 0.02 + 0.005),
        "zeros": _w(torch.full((ng, F), qmax / 2.0, dtype=float)),
        "U": _w(U.to(float)),
        "V": _w(V.to(float)),
    }

def reference(x, qw, scales, zeros, U, V):
    B, S, D = x.shape
    ng, F = scales.shape
    G = D // ng
    s = (lambda x, n, axis: torch.repeat_interleave(x, n, dim=axis))(scales, G, axis=0)
    z = (lambda x, n, axis: torch.repeat_interleave(x, n, dim=axis))(zeros, G, axis=0)
    wq = ((lambda x, dt: x.to(dt))(qw, x.dtype) - z) * s
    xr = torch.einsum("bsd,de->bse", x, U)
    h  = torch.einsum("bse,ef->bsf", xr, wq)
    y  = torch.einsum("bsf,fg->bsg", h, V)
    return y
```

```cpp
#include <hip/hip_runtime.h>
#include <hip/hip_bf16.h>
#include <stdint.h>

typedef unsigned v4u __attribute__((ext_vector_type(4)));
typedef float f32x4 __attribute__((ext_vector_type(4)));
typedef __bf16 bf16x8 __attribute__((ext_vector_type(8)));
typedef __bf16 bf16x2 __attribute__((ext_vector_type(2)));
typedef float f32x2 __attribute__((ext_vector_type(2)));

constexpr int M = 64, D = 1024, F = 3584, G = 32;

__device__ __forceinline__ f32x4 mfma(v4u a, v4u b, f32x4 c) {
  return __builtin_amdgcn_mfma_f32_16x16x32_bf16(__builtin_bit_cast(bf16x8, a), __builtin_bit_cast(bf16x8, b), c, 0, 0, 0);
}
__device__ __forceinline__ unsigned pk2(float lo, float hi) {
  f32x2 v = {lo, hi};
  bf16x2 r = __builtin_convertvector(v, bf16x2);
  return __builtin_bit_cast(unsigned, r);
}
__device__ __forceinline__ float rbf(float x) {
  return __builtin_bit_cast(float, pk2(x, x) << 16);
}

template <int K, int N, int NW>
__global__ __launch_bounds__(NW * 64) void gemm_bf16(const unsigned short* __restrict__ A, const unsigned short* __restrict__ B,
                                                     unsigned short* __restrict__ C) {
  __shared__ f32x4 red[NW][8][64];
  const int lane = threadIdx.x & 63, w = threadIdx.x >> 6;
  const int n0 = blockIdx.x * 32;
  constexpr int KW = K / NW;
  f32x4 acc[4][2];
  for (int i = 0; i < 4; ++i) for (int j = 0; j < 2; ++j) acc[i][j] = f32x4{0, 0, 0, 0};
  const int kb = w * KW + 8 * (lane >> 4);
#pragma unroll 2
  for (int ks = 0; ks < KW; ks += 32) {
    unsigned b[8];
#pragma unroll
    for (int j = 0; j < 8; ++j) b[j] = *(const unsigned*)(B + (size_t)(kb + ks + j) * N + n0 + 2 * (lane & 15));
    v4u a[4];
#pragma unroll
    for (int mt = 0; mt < 4; ++mt) a[mt] = *(const v4u*)(A + (size_t)(mt * 16 + (lane & 15)) * K + kb + ks);
    v4u b0, b1;
#pragma unroll
    for (int j = 0; j < 4; ++j) {
      b0[j] = __builtin_amdgcn_perm(b[2 * j + 1], b[2 * j], 0x05040100);
      b1[j] = __builtin_amdgcn_perm(b[2 * j + 1], b[2 * j], 0x07060302);
    }
#pragma unroll
    for (int mt = 0; mt < 4; ++mt) {
      acc[mt][0] = mfma(a[mt], b0, acc[mt][0]);
      acc[mt][1] = mfma(a[mt], b1, acc[mt][1]);
    }
  }
  for (int mt = 0; mt < 4; ++mt) for (int j = 0; j < 2; ++j) red[w][mt * 2 + j][lane] = acc[mt][j];
  __syncthreads();
  if (w < 4) {
    const int mt = w;
    f32x4 s0 = {0, 0, 0, 0}, s1 = {0, 0, 0, 0};
    for (int ww = 0; ww < NW; ++ww) { s0 += red[ww][mt * 2][lane]; s1 += red[ww][mt * 2 + 1][lane]; }
    for (int r = 0; r < 4; ++r) {
      const int row = mt * 16 + (lane >> 4) * 4 + r;
      *(unsigned*)(C + (size_t)row * N + n0 + 2 * (lane & 15)) = pk2(s0[r], s1[r]);
    }
  }
}

template <int NW>
__global__ __launch_bounds__(NW * 64) void gemm_q(const unsigned short* __restrict__ A, const int* __restrict__ qw,
                                                  const unsigned short* __restrict__ sc, const unsigned short* __restrict__ zr,
                                                  unsigned short* __restrict__ C) {
  __shared__ f32x4 red[NW][4][64];
  const int lane = threadIdx.x & 63, w = threadIdx.x >> 6;
  const int n = blockIdx.x * 16 + (lane & 15);
  constexpr int KW = D / NW;
  f32x4 acc[4];
  for (int i = 0; i < 4; ++i) acc[i] = f32x4{0, 0, 0, 0};
  const int kb = w * KW + 8 * (lane >> 4);
#pragma unroll
  for (int ks = 0; ks < KW; ks += 32) {
    int q[8];
#pragma unroll
    for (int j = 0; j < 8; ++j) q[j] = qw[(size_t)(kb + ks + j) * F + n];
    const int g = (w * KW + ks) / G;
    const float s = __builtin_bit_cast(float, (unsigned)sc[g * F + n] << 16);
    const float z = __builtin_bit_cast(float, (unsigned)zr[g * F + n] << 16);
    v4u a[4];
#pragma unroll
    for (int mt = 0; mt < 4; ++mt) a[mt] = *(const v4u*)(A + (size_t)(mt * 16 + (lane & 15)) * D + kb + ks);
    v4u b;
#pragma unroll
    for (int j = 0; j < 4; ++j) {
      const float d0 = rbf(rbf((float)q[2 * j]) - z), d1 = rbf(rbf((float)q[2 * j + 1]) - z);
      b[j] = pk2(d0 * s, d1 * s);
    }
#pragma unroll
    for (int mt = 0; mt < 4; ++mt) acc[mt] = mfma(a[mt], b, acc[mt]);
  }
  for (int mt = 0; mt < 4; ++mt) red[w][mt][lane] = acc[mt];
  __syncthreads();
  if (w < 4) {
    const int mt = w;
    f32x4 s0 = {0, 0, 0, 0};
    for (int ww = 0; ww < NW; ++ww) s0 += red[ww][mt][lane];
    for (int r = 0; r < 4; ++r) {
      const int row = mt * 16 + (lane >> 4) * 4 + r;
      C[(size_t)row * F + n] = (unsigned short)(pk2(s0[r], s0[r]) & 0xffff);
    }
  }
}

extern "C" void kernel_launch(void* const* d_in, const int* in_sizes, int n_in, void* d_out, int out_size, void* d_ws,
                              size_t ws_size, hipStream_t stream) {
  const unsigned short* x = (const unsigned short*)d_in[0];
  const int* qw = (const int*)d_in[1];
  const unsigned short* sc = (const unsigned short*)d_in[2];
  const unsigned short* zr = (const unsigned short*)d_in[3];
  const unsigned short* U = (const unsigned short*)d_in[4];
  const unsigned short* V = (const unsigned short*)d_in[5];
  unsigned short* xr = (unsigned short*)d_ws;
  unsigned short* h = xr + M * D;
  gemm_bf16<D, D, 16><<<D / 32, 1024, 0, stream>>>(x, U, xr);
  gemm_q<8><<<F / 16, 512, 0, stream>>>(xr, qw, sc, zr, h);
  gemm_bf16<F, F, 16><<<F / 32, 1024, 0, stream>>>(h, V, (unsigned short*)d_out);
}
```

```cpp
#include <hip/hip_runtime.h>
#include <stdint.h>

typedef unsigned v4u __attribute__((ext_vector_type(4)));
typedef unsigned v2u __attribute__((ext_vector_type(2)));
typedef float f32x4 __attribute__((ext_vector_type(4)));
typedef float f32x2 __attribute__((ext_vector_type(2)));
typedef __bf16 bf16x8 __attribute__((ext_vector_type(8)));
typedef __bf16 bf16x2 __attribute__((ext_vector_type(2)));
typedef short v4s __attribute__((ext_vector_type(4)));
#define LAS __attribute__((address_space(3)))
#define GAS __attribute__((address_space(1)))
typedef __amdgpu_buffer_rsrc_t rsrc_t;

#define AUX_Q 0
#ifndef NVA
#define NVA 4
#endif
#ifndef NV_KEEP
#define NV_KEEP 3
#endif
#define FLAG1_AT 20

constexpr int D = 1024, F = 3584;
constexpr int WS_EPOCH = 0, WS_F1 = 4096, WS_F2 = WS_F1 + 128 * 128, WS_XR = 65536, WS_H = WS_XR + 131072, WS_P = WS_H + 458752,
              WS_END = WS_P + 56 * 8 * 6144;
constexpr int LDS_V = 0, LDS_RED = 112 * 1024, LDS_BYTES = LDS_RED + 32 * 1024;
constexpr int SPIN_MAX = 1 << 18;

#define STAMP(i) do {} while (0)

__device__ __forceinline__ f32x4 mfma(v4u a, v4u b, f32x4 c) {
  return __builtin_amdgcn_mfma_f32_16x16x32_bf16(__builtin_bit_cast(bf16x8, a), __builtin_bit_cast(bf16x8, b), c, 0, 0, 0);
}
__device__ __forceinline__ unsigned pk2(float lo, float hi) {
  f32x2 v = {lo, hi};
  return __builtin_bit_cast(unsigned, __builtin_convertvector(v, bf16x2));
}
__device__ __forceinline__ float lo16(unsigned u) { return __builtin_bit_cast(float, u << 16); }
__device__ __forceinline__ float hi16(unsigned u) { return __builtin_bit_cast(float, u & 0xffff0000u); }
__device__ __forceinline__ unsigned unpoison(unsigned u) { return u == 0xffffffffu ? 0x7fff7fffu : u; }
__device__ __forceinline__ v2u trread(unsigned addr) {
  return __builtin_bit_cast(v2u, __builtin_amdgcn_ds_read_tr16_b64_v4i16((LAS v4s*)(uintptr_t)addr));
}
template <int NT> __device__ __forceinline__ void dma16(v4u rs, unsigned voff, unsigned soff, unsigned lds) {
  if (NT) asm volatile("s_mov_b32 m0, %3\n s_nop 0\n buffer_load_dwordx4 %0, %1, %2 offen nt lds" :: "v"(voff), "s"(rs), "s"(soff), "s"(lds) : "memory");
  else asm volatile("s_mov_b32 m0, %3\n s_nop 0\n buffer_load_dwordx4 %0, %1, %2 offen lds" :: "v"(voff), "s"(rs), "s"(soff), "s"(lds) : "memory");
}
__device__ __forceinline__ v4u mkrs(const void* p, unsigned bytes) {
  const uint64_t a = (uint64_t)p;
  return v4u{(unsigned)a, (unsigned)(a >> 32) & 0xffffu, bytes, 0x00020000u};
}
__device__ __forceinline__ rsrc_t mk(const void* p, int bytes) { return __builtin_amdgcn_make_buffer_rsrc((void*)p, 0, bytes, 0x00020000); }
#define BAR() do { asm volatile("s_waitcnt lgkmcnt(0)" ::: "memory"); __builtin_amdgcn_s_barrier(); asm volatile("" ::: "memory"); } while (0)

__device__ __forceinline__ void wait_flags(rsrc_t rw, int off, v2u tag) {
  for (int spin = 0; spin < SPIN_MAX; ++spin) {
    asm volatile("" ::: "memory");
    const v2u f = __builtin_amdgcn_raw_buffer_load_b64(rw, off, 0, 16);
    if (__all(f.x == tag.x && f.y == tag.y)) break;
  }
  asm volatile("" ::: "memory");
}
__device__ __forceinline__ unsigned max4(v4u v) { return max(max(v.x, v.y), max(v.z, v.w)); }

__global__ __launch_bounds__(512, 2) void quip_kernel(const void* __restrict__ x, const void* __restrict__ qw, const void* __restrict__ sc,
                                                      const void* __restrict__ zr, const void* __restrict__ U, const void* __restrict__ V,
                                                      void* y, void* ws) {
  __shared__ __attribute__((aligned(1024))) unsigned char smem[LDS_BYTES];
  const int tid = threadIdx.x, lane = tid & 63, w = __builtin_amdgcn_readfirstlane(tid >> 6), c = blockIdx.x;
  const int xcd = c & 7, r = c >> 3;
  const unsigned lds0 = (unsigned)(uintptr_t)(LAS unsigned char*)smem;
  const rsrc_t rw = mk(ws, WS_END);
  const v2u ep = *(const __attribute__((address_space(4))) v2u*)(uintptr_t)ws;
  const v2u tag = {((ep.x & 0x7f7f7f7fu) + 0x01010101u) ^ (ep.x & 0x80808080u), ((ep.y & 0x7f7f7f7fu) + 0x01010101u) ^ (ep.y & 0x80808080u)};
  const v4u poison = {~0u, ~0u, ~0u, ~0u};
  const int g = lane >> 4, m = lane & 15, q = (lane >> 2) & 3, p = lane & 3;

  if (r < 4) {
    const int ct = xcd * 4 + r;
    if (w < 4) {
      __builtin_amdgcn_raw_buffer_store_b128(poison, rw, WS_XR + (ct * 4 + w) * 1024 + lane * 16, 0, 16);
      const v4u ru = mkrs(U, D * D * 2);
      const unsigned voff = (unsigned)((256 * w + (lane >> 2)) * 2048 + ct * 64 + (((lane & 3) ^ (((lane >> 5) & 1) << 1)) * 16));
#pragma unroll
      for (int j = 0; j < 16; ++j) dma16<0>(ru, voff, j * 16 * 2048, lds0 + (16 * w + j) * 1024);
      asm volatile("s_waitcnt vmcnt(0)" ::: "memory");
      BAR();
      if (c == 0 && w == 0) {
#pragma unroll 1
        for (int j = 0; j < 6; ++j) { const int f = j * 64 + lane; wait_flags(rw, WS_F1 + (f < 352 ? f : 0) * 128, tag); }
        if (lane == 0) __builtin_amdgcn_raw_buffer_store_b64(tag, rw, WS_EPOCH, 0, 16);
      }
    } else {
      const int mt = w - 4;
      const int xo = WS_XR + (ct * 4 + mt) * 1024 + lane * 16;
      const rsrc_t rx = mk(x, 64 * D * 2);
      v4u xf[32];
#pragma unroll
      for (int s = 0; s < 32; ++s) xf[s] = __builtin_amdgcn_raw_buffer_load_b128(rx, (16 * mt + m) * 2048 + g * 16 + s * 64, 0, 0);
      __builtin_amdgcn_sched_barrier(0);
      BAR();
      STAMP(1);
      const unsigned a0 = lds0 + (8 * g + q) * 64 + (g & 1) * 32 + p * 8, a1 = a0 ^ 32;
      f32x4 acc0 = {0, 0, 0, 0}, acc1 = {0, 0, 0, 0};
#pragma unroll
      for (int s = 0; s < 32; ++s) {
        const v2u u00 = trread(a0 + s * 2048), u01 = trread(a0 + s * 2048 + 256), u10 = trread(a1 + s * 2048), u11 = trread(a1 + s * 2048 + 256);
        acc0 = mfma(v4u{u00.x, u00.y, u01.x, u01.y}, xf[s], acc0);
        acc1 = mfma(v4u{u10.x, u10.y, u11.x, u11.y}, xf[s], acc1);
        if (s == FLAG1_AT) {
          __builtin_amdgcn_sched_barrier(0);
          if (lane == 0) __builtin_amdgcn_raw_buffer_store_b64(tag, rw, WS_F1 + (ct * 4 + mt) * 128, 0, 16);
          __builtin_amdgcn_sched_barrier(0);
        }
      }
      const v4u o = {unpoison(pk2(acc0[0], acc0[1])), unpoison(pk2(acc0[2], acc0[3])), unpoison(pk2(acc1[0], acc1[1])), unpoison(pk2(acc1[2], acc1[3]))};
      __builtin_amdgcn_raw_buffer_store_b128(o, rw, xo, 0, 16);
      STAMP(2);
    }
    return;
  }

  const int rr = r - 4, ct2 = xcd * 14 + (rr >> 1), rh = rr & 1, ks = xcd >> 1, ns = (xcd & 1) * 28 + rr;
  const int mt3 = (w >> 2) * 2 + (w & 1), kh = (w >> 1) & 1;
  const bool owner = (w & 3) == ks;
  const int ho = WS_H + (ct2 * 4 + 2 * rh + w) * 1024 + lane * 16;
  const int po = WS_P + (ns * 8 + w) * 6144;
  if (w < 2) __builtin_amdgcn_raw_buffer_store_b128(poison, rw, ho, 0, 16);
  if (!owner && lane < 16) __builtin_amdgcn_raw_buffer_store_b32(~tag.x, rw, po + ((ks - (w & 3) - 1) & 3) * 2048 + lane * 128, 0, 16);
  const v4u rv = mkrs(V, F * F * 2);
  const unsigned vvoff = (unsigned)((896 * ks + 4 * (lane >> 3)) * (F * 2) + ns * 128 + (((lane & 7) ^ (((lane >> 4) & 3) << 1)) * 16));
#define VPIECE(j) do { const int pc = 14 * w + (j); dma16<((j) < 14 - NV_KEEP)>(rv, vvoff, (unsigned)(((pc >> 2) * 32 + (pc & 3)) * (F * 2)), lds0 + LDS_V + pc * 1024); } while (0)
#define VRANGE(lo, hi) do { \
    if ((lo) <= 0 && 0 < (hi)) VPIECE(0); if ((lo) <= 1 && 1 < (hi)) VPIECE(1); if ((lo) <= 2 && 2 < (hi)) VPIECE(2); if ((lo) <= 3 && 3 < (hi)) VPIECE(3); \
    if ((lo) <= 4 && 4 < (hi)) VPIECE(4); if ((lo) <= 5 && 5 < (hi)) VPIECE(5); if ((lo) <= 6 && 6 < (hi)) VPIECE(6); if ((lo) <= 7 && 7 < (hi)) VPIECE(7); \
    if ((lo) <= 8 && 8 < (hi)) VPIECE(8); if ((lo) <= 9 && 9 < (hi)) VPIECE(9); if ((lo) <= 10 && 10 < (hi)) VPIECE(10); if ((lo) <= 11 && 11 < (hi)) VPIECE(11); \
    if ((lo) <= 12 && 12 < (hi)) VPIECE(12); if ((lo) <= 13 && 13 < (hi)) VPIECE(13); } while (0)
  VRANGE(0, NVA);

  const int pb = (lane >> 3) & 1, cg = lane & 7;
  const rsrc_t rq = mk(qw, D * F * 4), rs = mk(sc, 32 * F * 2), rz = mk(zr, 32 * F * 2);
  v2u sv[2], zv[2];
#pragma unroll
  for (int hh = 0; hh < 2; ++hh) {
    const int o = ((4 * w + 2 * hh + pb) * F + 32 * ct2 + 4 * cg) * 2;
    sv[hh] = __builtin_amdgcn_raw_buffer_load_b64(rs, o, 0, 0);
    zv[hh] = __builtin_amdgcn_raw_buffer_load_b64(rz, o, 0, 0);
  }
  v4u qv[16];
  const int qo = (128 * w + 32 * pb + 4 * g) * (F * 4) + (32 * ct2 + 4 * cg) * 4;
#pragma unroll
  for (int i = 0; i < 16; ++i) qv[i] = __builtin_amdgcn_raw_buffer_load_b128(rq, qo, (64 * (i >> 3) + 16 * ((i >> 2) & 1) + (i & 3)) * (F * 4), AUX_Q);
  __builtin_amdgcn_sched_barrier(0);
  STAMP(1);
  v4u T[2][2][2];
#pragma unroll
  for (int hh = 0; hh < 2; ++hh) {
    const float z[4] = {lo16(zv[hh].x), hi16(zv[hh].x), lo16(zv[hh].y), hi16(zv[hh].y)};
    const float s[4] = {lo16(sv[hh].x), hi16(sv[hh].x), lo16(sv[hh].y), hi16(sv[hh].y)};
#pragma unroll
    for (int e2 = 0; e2 < 4; ++e2) {
      unsigned P[4];
#pragma unroll
      for (int j = 0; j < 4; ++j) {
        const unsigned qq = pk2((float)(int)qv[hh * 8 + 2 * e2][j], (float)(int)qv[hh * 8 + 2 * e2 + 1][j]);
        const unsigned dd = pk2(lo16(qq) - z[j], hi16(qq) - z[j]);
        P[j] = pk2(lo16(dd) * s[j], hi16(dd) * s[j]);
      }
#pragma unroll
      for (int t = 0; t < 2; ++t) {
        const unsigned r1 = __builtin_amdgcn_update_dpp(P[2 * t], P[2 * t + 1], 0x128, 0xf, 0xc, false);
        const unsigned r0 = __builtin_amdgcn_update_dpp(P[2 * t + 1], P[2 * t], 0x128, 0xf, 0x3, false);
        T[hh][0][t][e2] = r1;
        T[hh][1][t][e2] = r0;
      }
    }
  }
  STAMP(2);
  wait_flags(rw, WS_F1 + ((4 * w + (lane & 3)) * 4 + 2 * rh + ((lane >> 2) & 1)) * 128, tag);
  STAMP(3);
  v4u xf[8];
  const int xo = WS_XR + (4 * w * 4 + 2 * rh) * 1024 + lane * 16;
  for (int spin = 0; spin < SPIN_MAX; ++spin) {
    asm volatile("" ::: "memory");
#pragma unroll
    for (int i = 0; i < 8; ++i) xf[i] = __builtin_amdgcn_raw_buffer_load_b128(rw, xo, (i >> 1) * 4096 + (i & 1) * 1024, 16);
    __builtin_amdgcn_sched_barrier(0);
    unsigned mx = 0;
#pragma unroll
    for (int i = 0; i < 8; ++i) mx = max(mx, max4(xf[i]));
    if (__all(mx != 0xffffffffu)) break;
  }
  STAMP(4);
  if (w >= 2) VRANGE(NVA, 14);
  f32x4 acc[2][2] = {{{0, 0, 0, 0}, {0, 0, 0, 0}}, {{0, 0, 0, 0}, {0, 0, 0, 0}}};
#pragma unroll
  for (int i = 0; i < 8; ++i)
#pragma unroll
    for (int t = 0; t < 2; ++t) acc[i & 1][t] = mfma(T[i >> 2][(i >> 1) & 1][t], xf[i], acc[i & 1][t]);
  LAS f32x4* red = (LAS f32x4*)(uintptr_t)(lds0 + LDS_RED);
#pragma unroll
  for (int i = 0; i < 4; ++i) red[(w * 4 + i) * 64 + lane] = acc[i >> 1][i & 1];
  BAR();
  STAMP(5);
  if (w < 2) {
    if (w == 0 && lane == 0) __builtin_amdgcn_raw_buffer_store_b64(tag, rw, WS_F2 + (ct2 * 2 + rh) * 128, 0, 16);
    f32x4 s0 = red[(w * 2) * 64 + lane], s1 = red[(w * 2 + 1) * 64 + lane];
#pragma unroll
    for (int ww = 1; ww < 8; ++ww) { s0 += red[(ww * 4 + w * 2) * 64 + lane]; s1 += red[(ww * 4 + w * 2 + 1) * 64 + lane]; }
    const v4u o = {unpoison(pk2(s0[0], s0[1])), unpoison(pk2(s0[2], s0[3])), unpoison(pk2(s1[0], s1[1])), unpoison(pk2(s1[2], s1[3]))};
    __builtin_amdgcn_raw_buffer_store_b128(o, rw, ho, 0, 16);
    VRANGE(NVA, 14);
  }
  STAMP(6);
  wait_flags(rw, WS_F2 + ((28 * ks + 14 * kh + (m < 14 ? m : 0)) * 2 + (w >> 2)) * 128, tag);
  STAMP(7);
  v4u hf[14];
  const int hg = WS_H + ((28 * ks + 14 * kh) * 4 + mt3) * 1024 + lane * 16;
#pragma unroll
  for (int i = 0; i < 14; ++i) hf[i] = __builtin_amdgcn_raw_buffer_load_b128(rw, hg, i * 4096, 16);
  __builtin_amdgcn_sched_barrier(0);
  BAR();
  for (int spin = 0; spin < SPIN_MAX; ++spin) {
    unsigned mx = 0;
#pragma unroll
    for (int i = 0; i < 14; ++i) mx = max(mx, max4(hf[i]));
    if (__all(mx != 0xffffffffu)) break;
    asm volatile("" ::: "memory");
#pragma unroll
    for (int i = 0; i < 14; ++i) hf[i] = __builtin_amdgcn_raw_buffer_load_b128(rw, hg, i * 4096, 16);
    __builtin_amdgcn_sched_barrier(0);
  }
  STAMP(8);
  f32x4 a3[4] = {{0, 0, 0, 0}, {0, 0, 0, 0}, {0, 0, 0, 0}, {0, 0, 0, 0}};
  const unsigned vb = lds0 + LDS_V + kh * 14 * 4096 + (g >> 1) * 1024 + (4 * (g & 1) + q) * 128 + p * 8;
  const unsigned sw = 2 * (g & 1) + (q >> 1);
#pragma unroll
  for (int s = 0; s < 14; ++s)
#pragma unroll
    for (int t = 0; t < 4; ++t) {
      const unsigned a = vb + ((t ^ sw) * 32) + s * 4096;
      const v2u b0 = trread(a), b1 = trread(a + 2048);
      a3[t] = mfma(v4u{b0.x, b0.y, b1.x, b1.y}, hf[s], a3[t]);
    }
  STAMP(9);
  red[(w * 2) * 64 + lane] = kh ? a3[0] : a3[2];
  red[(w * 2 + 1) * 64 + lane] = kh ? a3[1] : a3[3];
  BAR();
  f32x4 o0 = (kh ? a3[2] : a3[0]) + red[((w ^ 2) * 2) * 64 + lane];
  f32x4 o1 = (kh ? a3[3] : a3[1]) + red[((w ^ 2) * 2 + 1) * 64 + lane];
  const v4u tb = {tag.x & 0xffu, (tag.x >> 8) & 0xffu, (tag.x >> 16) & 0xffu, tag.x >> 24};
  STAMP(10);
  if (!owner) {
    const int o = po + ((ks - (w & 3) - 1) & 3) * 2048 + lane * 16;
    __builtin_amdgcn_raw_buffer_store_b128((__builtin_bit_cast(v4u, o0) & 0xffffff00u) | tb, rw, o, 0, 16);
    __builtin_amdgcn_raw_buffer_store_b128((__builtin_bit_cast(v4u, o1) & 0xffffff00u) | tb, rw, o + 1024, 0, 16);
    return;
  }
  v4u pf[6];
  for (int spin = 0; spin < SPIN_MAX; ++spin) {
    asm volatile("" ::: "memory");
#pragma unroll
    for (int i = 0; i < 6; ++i) pf[i] = __builtin_amdgcn_raw_buffer_load_b128(rw, po + lane * 16, i * 1024, 16);
    __builtin_amdgcn_sched_barrier(0);
    v4u bad = pf[0] ^ tb;
#pragma unroll
    for (int i = 1; i < 6; ++i) bad |= pf[i] ^ tb;
    if (__all(((bad.x | bad.y | bad.z | bad.w) & 0xffu) == 0)) break;
  }
#pragma unroll
  for (int i = 0; i < 3; ++i) {
    o0 += __builtin_bit_cast(f32x4, (pf[2 * i] & 0xffffff00u));
    o1 += __builtin_bit_cast(f32x4, (pf[2 * i + 1] & 0xffffff00u));
  }
  const rsrc_t ry = mk(y, 64 * F * 2);
  const int yo = ((16 * mt3 + m) * F + 64 * ns + 32 * kh + 4 * g) * 2;
  __builtin_amdgcn_raw_buffer_store_b64(v2u{pk2(o0[0], o0[1]), pk2(o0[2], o0[3])}, ry, yo, 0, 0);
  __builtin_amdgcn_raw_buffer_store_b64(v2u{pk2(o1[0], o1[1]), pk2(o1[2], o1[3])}, ry, yo + 32, 0, 0);
}

extern "C" void kernel_launch(void* const* d_in, const int* in_sizes, int n_in, void* d_out, int out_size, void* d_ws, size_t ws_size,
                              hipStream_t stream) {
  quip_kernel<<<256, 512, 0, stream>>>(d_in[0], d_in[1], d_in[2], d_in[3], d_in[4], d_in[5], d_out, d_ws);
}
```

```cpp
#include <hip/hip_runtime.h>
#include <stdint.h>

__global__ __launch_bounds__(320, 2) __attribute__((aligned(4096))) void quip_kernel(
    const void* x, unsigned xn, unsigned xf, const void* qw, unsigned qn, unsigned qf, const void* sc, unsigned sn, unsigned sf,
    const void* zr, unsigned zn, unsigned zf, const void* U, unsigned un, unsigned uf, const void* V, unsigned vn, unsigned vf,
    void* y, unsigned yn, unsigned yf, void* ws, unsigned wn, unsigned wf) {
  __shared__ __attribute__((aligned(1024))) unsigned char smem[163840];
  asm volatile(
      ".Lq_first:\n"
      "s_load_dwordx16 s[4:19], s[0:1], 0x0\n"
      "s_load_dwordx16 s[20:35], s[0:1], 0x40\n"
      "v_lshrrev_b32 v1, 6, v0\n"
      "v_and_b32 v0, 63, v0\n"
      "s_and_b32 s37, s2, 7\n"
      "v_readfirstlane_b32 s36, v1\n"
      "s_lshr_b32 s38, s2, 3\n"
      "v_lshlrev_b32 v3, 4, v0\n"
      "v_mov_b32 v6, 161792\n"
      "v_lshl_add_u32 v7, v1, 8, v6\n"
      "v_lshl_add_u32 v7, v0, 2, v7\n"
      "v_mov_b32 v2, 0\n"
      "ds_write_b32 v7, v2\n"
      "s_cmp_lt_u32 s38, 14\n"
      "s_cbranch_scc0 .Lq_b\n"
      "s_mov_b32 s40, s38\n"
      "s_cmp_eq_u32 s36, 4\n"
      "s_cbranch_scc1 .Lq_aload\n"
      "s_mov_b32 s56, 0x432180dc\n"
      "s_mov_b32 s57, 0xba9765\n"
      "s_lshl_b32 s50, s40, 2\n"
      "s_lshr_b64 s[56:57], s[56:57], s50\n"
      "s_and_b32 s39, s56, 15\n"
      "v_bfe_u32 v1, v0, 3, 1\n"
      "v_lshrrev_b32 v2, 4, v0\n"
      "v_and_b32 v4, 7, v0\n"
      "v_lshlrev_b32 v2, 2, v2\n"
      "v_lshl_add_u32 v2, v1, 5, v2\n"
      "s_mul_i32 s50, s36, 3670016\n"
      "v_mul_u32_u24 v2, 14336, v2\n"
      "v_lshl_add_u32 v2, v4, 4, v2\n"
      "v_add_u32 v8, s50, v2\n"
      "v_add_u32 v9, 14336, v8\n"
      "v_add_u32 v10, 28672, v8\n"
      "v_add_u32 v11, 43008, v8\n"
      "v_add_u32 v12, 229376, v8\n"
      "v_add_u32 v13, 243712, v8\n"
      "v_add_u32 v14, 258048, v8\n"
      "v_add_u32 v15, 272384, v8\n"
      "s_mul_i32 s50, s36, 57344\n"
      "v_mul_u32_u24 v1, 7168, v1\n"
      "v_lshl_add_u32 v1, v4, 3, v1\n"
      "v_add_u32 v1, s50, v1\n"
      "s_mul_i32 s51, s37, 14\n"
      "s_waitcnt lgkmcnt(0)\n"
      "s_lshr_b32 s50, s8, 7\n"
      "s_add_u32 s50, s50, s51\n"
      "s_sub_u32 s50, 3, s50\n"
      "s_and_b32 s50, s50, 7\n"
      "s_add_u32 s39, s39, s50\n"
      "s_cmp_ge_u32 s39, 14\n"
      "s_cselect_b32 s50, 14, 0\n"
      "s_sub_u32 s39, s39, s50\n"
      "s_add_u32 s39, s39, s51\n"
      "s_lshl_b32 s46, s39, 7\n"
      "s_sleep 6\n"
      "buffer_load_dwordx4 v[16:19], v8, s[8:11], s46 offen nt\n"
      "buffer_load_dwordx4 v[20:23], v9, s[8:11], s46 offen nt\n"
      "s_lshl_b32 s52, s39, 6\n"
      "buffer_load_dwordx2 v[144:145], v1, s[12:15], s52 offen\n"
      "buffer_load_dwordx2 v[146:147], v1, s[16:19], s52 offen\n"
      "buffer_load_dwordx4 v[24:27], v10, s[8:11], s46 offen nt\n"
      "buffer_load_dwordx4 v[28:31], v11, s[8:11], s46 offen nt\n"
      "buffer_load_dwordx4 v[32:35], v12, s[8:11], s46 offen nt\n"
      "buffer_load_dwordx4 v[36:39], v13, s[8:11], s46 offen nt\n"
      "buffer_load_dwordx4 v[40:43], v14, s[8:11], s46 offen nt\n"
      "buffer_load_dwordx4 v[44:47], v15, s[8:11], s46 offen nt\n"
      "s_add_u32 s47, s46, 917504\n"
      "s_add_u32 s52, s52, 14336\n"
      "buffer_load_dwordx2 v[148:149], v1, s[12:15], s52 offen\n"
      "buffer_load_dwordx2 v[150:151], v1, s[16:19], s52 offen\n"
      "s_add_u32 s48, s46, 1835008\n"
      "s_add_u32 s52, s52, 14336\n"
      "buffer_load_dwordx2 v[152:153], v1, s[12:15], s52 offen\n"
      "buffer_load_dwordx2 v[154:155], v1, s[16:19], s52 offen\n"
      "s_add_u32 s49, s46, 2752512\n"
      "s_add_u32 s52, s52, 14336\n"
      "buffer_load_dwordx2 v[156:157], v1, s[12:15], s52 offen\n"
      "buffer_load_dwordx2 v[158:159], v1, s[16:19], s52 offen\n"
      "buffer_load_dwordx4 v[48:51], v8, s[8:11], s47 offen nt\n"
      "buffer_load_dwordx4 v[52:55], v9, s[8:11], s47 offen nt\n"
      "s_branch .Lq_amain\n"
      ".Lq_aload:\n"
      "s_setprio 3\n"
      "ds_write_b32 v7, v2 offset:256\n"
      "v_lshlrev_b32 v1, 11, v0\n"
      "s_lshl_b32 s51, s37, 1\n"
      "s_waitcnt lgkmcnt(0)\n"
      "s_lshr_b32 s50, s20, 7\n"
      "s_add_u32 s50, s50, s51\n"
      "s_sub_u32 s50, 3, s50\n"
      "s_and_b32 s50, s50, 7\n"
      "s_cmp_lt_u32 s50, 2\n"
      "s_cbranch_scc0 .Lq_ut1\n"
      "s_add_u32 s50, s50, s51\n"
      "s_lshl_b32 s50, s50, 7\n"
      "s_lshl_b32 s51, s38, 17\n"
      "s_add_u32 s50, s50, s51\n"
      "buffer_load_dword v20, v1, s[20:23], s50 offen\n"
      ".Lq_ut1:\n"
      "s_load_dwordx2 s[44:45], s[32:33], 0x0\n"
      "s_barrier\n"
      "s_branch .Lq_aload2\n"
      ".Lq_b:\n"
      "s_sub_u32 s40, s38, 14\n"
      "s_cmp_lt_u32 s38, 16\n"
      "s_cbranch_scc1 .Lq_mark\n"
      "s_cmp_eq_u32 s36, 4\n"
      "s_cbranch_scc1 .Lq_bload\n"
      "s_cmp_lt_u32 s38, 24\n"
      "s_cbranch_scc1 .Lq_help\n"
      "v_lshrrev_b32 v1, 2, v0\n"
      "v_and_b32 v2, 3, v0\n"
      "v_lshrrev_b32 v4, 4, v0\n"
      "v_and_b32 v5, 2, v4\n"
      "v_xor_b32 v5, v5, v2\n"
      "v_lshlrev_b32 v5, 4, v5\n"
      "v_lshl_add_u32 v8, v1, 11, v5\n"
      "v_sub_u32 v5, 0, v4\n"
      "v_xor_b32 v5, v5, v2\n"
      "v_and_b32 v5, 3, v5\n"
      "v_lshlrev_b32 v5, 4, v5\n"
      "v_lshl_add_u32 v12, v1, 11, v5\n"
      "v_add_u32 v13, 64, v12\n"
      "v_add_u32 v14, 31744, v12\n"
      "v_add_u32 v15, 30784, v12\n"
      "v_add_u32 v12, 0x400, v12\n"
      "v_add_u32 v9, 31744, v8\n"
      "v_add_u32 v10, 63488, v8\n"
      "v_add_u32 v11, 95232, v8\n"
      "s_and_b32 s50, s38, 3\n"
      "s_bfe_u32 s51, s38, 0x10002\n"
      "s_lshl_b32 s39, s37, 2\n"
      "s_add_u32 s39, s39, s50\n"
      "s_lshl_b32 s52, s51, 1\n"
      "s_add_u32 s52, s52, s50\n"
      "s_lshl_b32 s46, s51, 16\n"
      "s_lshl_b32 s50, s36, 9\n"
      "s_add_u32 s46, s46, s50\n"
      "s_lshl_b32 s47, s36, 19\n"
      "s_lshl_b32 s50, s39, 6\n"
      "s_add_u32 s47, s47, s50\n"
      "s_lshl_b32 s48, s36, 15\n"
      "s_mov_b32 m0, s48\n"
      "s_add_u32 s53, s52, 4\n"
      "s_waitcnt lgkmcnt(0)\n"
      "s_sub_u32 s4, s4, 0x400\n"
      "s_subb_u32 s5, s5, 0\n"
      "s_add_u32 s6, s6, 0x400\n"
      "s_and_b32 s50, s52, 3\n"
      "s_lshl_b32 s51, s50, 17\n"
      "s_add_u32 s51, s51, s47\n"
      "buffer_load_dwordx4 v8, s[20:23], s51 offen offset:0 lds\n"
      "buffer_load_dwordx4 v9, s[20:23], s51 offen offset:1024 lds\n"
      "buffer_load_dwordx4 v10, s[20:23], s51 offen offset:2048 lds\n"
      "buffer_load_dwordx4 v11, s[20:23], s51 offen offset:3072 lds\n"
      "s_add_u32 m0, m0, 0x1000\n"
      "s_lshl_b32 s50, s50, 7\n"
      "s_add_u32 s50, s50, s46\n"
      "buffer_load_dwordx4 v12, s[4:7], s50 offen offset:0 lds\n"
      "buffer_load_dwordx4 v13, s[4:7], s50 offen offset:1024 lds\n"
      "buffer_load_dwordx4 v14, s[4:7], s50 offen offset:2048 lds\n"
      "buffer_load_dwordx4 v15, s[4:7], s50 offen offset:3072 lds\n"
      "s_add_u32 m0, m0, 0x1000\n"
      "s_add_u32 s52, s52, 1\n"
      "s_branch .Lq_s1c\n"
      ".Lq_help:\n"
      "s_sub_u32 s50, s38, 16\n"
      "v_lshlrev_b32 v2, 7, v0\n"
      "s_lshl_b32 s50, s50, 1\n"
      "s_add_u32 s50, s50, s36\n"
      "s_lshl_b32 s50, s50, 13\n"
      "s_cmp_lt_u32 s36, 2\n"
      "s_cbranch_scc0 .Lq_hc\n"
      "s_waitcnt lgkmcnt(0)\n"
      "buffer_load_dword v20, v2, s[4:7], s50 offen\n"
      "s_branch .Lq_td\n"
      ".Lq_hc:\n"
      "s_getpc_b64 s[56:57]\n"
      ".Lq_pc:\n"
      "s_sub_u32 s56, s56, .Lq_pc-.Lq_first\n"
      "s_subb_u32 s57, s57, 0\n"
      "s_sub_u32 s50, s50, 0x4000\n"
      "s_lshr_b32 s50, s50, 2\n"
      "v_add_u32 v2, s50, v2\n"
      "v_cmp_gt_u32 vcc, .Lq_last-.Lq_first, v2\n"
      "s_and_b32 exec_lo, vcc_lo, 0xffff\n"
      "s_mov_b32 exec_hi, 0\n"
      "s_cbranch_execz .Lq_td\n"
      "global_load_dword v20, v2, s[56:57]\n"
      ".Lq_td:\n"
      "s_mov_b64 exec, -1\n"
      "s_waitcnt lgkmcnt(0)\n"
      "s_sub_u32 s52, s38, 2\n"
      "s_cmp_eq_u32 s36, 3\n"
      "s_cselect_b32 s50, s52, 99\n"
      "s_cmp_lt_u32 s50, 16\n"
      "s_cbranch_scc0 .Lq_td2\n"
      "v_lshlrev_b32 v1, 11, v0\n"
      "s_lshl_b32 s51, s37, 1\n"
      "s_waitcnt lgkmcnt(0)\n"
      "s_lshr_b32 s50, s20, 7\n"
      "s_add_u32 s50, s50, s51\n"
      "s_sub_u32 s50, 3, s50\n"
      "s_and_b32 s50, s50, 7\n"
      "s_cmp_lt_u32 s50, 2\n"
      "s_cbranch_scc0 .Lq_ut2\n"
      "s_add_u32 s50, s50, s51\n"
      "s_lshl_b32 s50, s50, 7\n"
      "s_lshl_b32 s51, s52, 17\n"
      "s_add_u32 s50, s50, s51\n"
      "buffer_load_dword v20, v1, s[20:23], s50 offen\n"
      ".Lq_ut2:\n"
      ".Lq_td2:\n"
      "s_barrier\n"
      "s_branch .Lq_bmain\n"
      ".Lq_mark:\n"
      "v_mov_b32 v8, -1\n"
      "v_mov_b32 v9, -1\n"
      "v_mov_b32 v10, -1\n"
      "v_mov_b32 v11, -1\n"
      "s_lshl_b32 s53, s37, 1\n"
      "s_add_u32 s53, s53, s40\n"
      "s_cmp_eq_u32 s36, 4\n"
      "s_cbranch_scc0 .Lq_markh\n"
      "s_setprio 3\n"
      "ds_write_b32 v7, v2 offset:256\n"
      "s_lshl_b32 s52, s53, 13\n"
      "s_add_u32 s52, s52, 65536\n"
      "s_add_u32 s51, s52, 0x1000\n"
      "s_lshl_b32 s53, s53, 7\n"
      "s_add_u32 s53, s53, 8192\n"
      "s_waitcnt lgkmcnt(0)\n"
      "s_load_dwordx2 s[44:45], s[32:33], 0x0\n"
      "buffer_store_dwordx4 v[8:11], v3, s[32:35], s52 offen offset:0 sc1\n"
      "buffer_store_dwordx4 v[8:11], v3, s[32:35], s52 offen offset:1024 sc1\n"
      "buffer_store_dwordx4 v[8:11], v3, s[32:35], s52 offen offset:2048 sc1\n"
      "buffer_store_dwordx4 v[8:11], v3, s[32:35], s52 offen offset:3072 sc1\n"
      "buffer_store_dwordx4 v[8:11], v3, s[32:35], s51 offen offset:0 sc1\n"
      "buffer_store_dwordx4 v[8:11], v3, s[32:35], s51 offen offset:1024 sc1\n"
      "buffer_store_dwordx4 v[8:11], v3, s[32:35], s51 offen offset:2048 sc1\n"
      "buffer_store_dwordx4 v[8:11], v3, s[32:35], s51 offen offset:3072 sc1\n"
      "s_waitcnt lgkmcnt(0)\n"
      "s_and_b32 s50, s44, 0x7f7f7f7f\n"
      "s_add_u32 s50, s50, 0x01010101\n"
      "s_and_b32 s51, s44, 0x80808080\n"
      "s_xor_b32 s44, s50, s51\n"
      "s_and_b32 s50, s45, 0x7f7f7f7f\n"
      "s_add_u32 s50, s50, 0x01010101\n"
      "s_and_b32 s51, s45, 0x80808080\n"
      "s_xor_b32 s45, s50, s51\n"
      "v_mov_b32 v12, s44\n"
      "v_mov_b32 v13, s45\n"
      "s_waitcnt vmcnt(0)\n"
      "s_mov_b64 exec, 1\n"
      "buffer_store_dwordx2 v[12:13], v0, s[32:35], s53 offen sc1\n"
      "s_mov_b64 exec, -1\n"
      "s_branch .Lq_bload3\n"
      ".Lq_markh:\n"
      "s_lshl_b32 s51, s36, 4\n"
      "s_add_u32 s51, s51, s53\n"
      "s_mul_i32 s51, s51, 7168\n"
      "s_add_u32 s51, s51, 196608\n"
      "s_add_u32 s52, s51, 0x1000\n"
      "s_waitcnt lgkmcnt(0)\n"
      "s_sleep 6\n"
      "buffer_store_dwordx4 v[8:11], v3, s[32:35], s51 offen offset:0 sc1\n"
      "buffer_store_dwordx4 v[8:11], v3, s[32:35], s51 offen offset:1024 sc1\n"
      "buffer_store_dwordx4 v[8:11], v3, s[32:35], s51 offen offset:2048 sc1\n"
      "buffer_store_dwordx4 v[8:11], v3, s[32:35], s51 offen offset:3072 sc1\n"
      "buffer_store_dwordx4 v[8:11], v3, s[32:35], s52 offen offset:0 sc1\n"
      "buffer_store_dwordx4 v[8:11], v3, s[32:35], s52 offen offset:1024 sc1\n"
      "buffer_store_dwordx4 v[8:11], v3, s[32:35], s52 offen offset:2048 sc1\n"
      "s_waitcnt vmcnt(0)\n"
      "s_barrier\n"
      "s_branch .Lq_bmain\n"
      ".Lq_bload:\n"
      "s_setprio 3\n"
      "ds_write_b32 v7, v2 offset:256\n"
      "s_waitcnt lgkmcnt(0)\n"
      "s_load_dwordx2 s[44:45], s[32:33], 0x0\n"
      "s_branch .Lq_bload2\n"
      ".Lq_s1c:\n"
      "v_and_b32 v1, 3, v1\n"
      "v_lshl_add_u32 v5, v4, 3, v1\n"
      "v_lshlrev_b32 v2, 3, v2\n"
      "v_lshl_add_u32 v2, v5, 6, v2\n"
      "v_and_b32 v5, 1, v4\n"
      "v_lshl_add_u32 v2, v5, 5, v2\n"
      "v_add_u32 v48, s48, v2\n"
      "v_xor_b32 v49, 32, v48\n"
      "v_sub_u32 v1, 0, v1\n"
      "v_xor_b32 v1, v1, v4\n"
      "v_and_b32 v1, 3, v1\n"
      "v_and_b32 v5, 15, v0\n"
      "v_lshl_add_u32 v1, v5, 2, v1\n"
      "v_lshl_add_u32 v50, v1, 4, s48\n"
      "v_mov_b32 v64, 0\n"
      "v_mov_b32 v65, 0\n"
      "v_mov_b32 v66, 0\n"
      "v_mov_b32 v67, 0\n"
      "v_mov_b32 v68, 0\n"
      "v_mov_b32 v69, 0\n"
      "v_mov_b32 v70, 0\n"
      "v_mov_b32 v71, 0\n"
      "v_mov_b32 v72, 0\n"
      "v_mov_b32 v73, 0\n"
      "v_mov_b32 v74, 0\n"
      "v_mov_b32 v75, 0\n"
      "v_mov_b32 v76, 0\n"
      "v_mov_b32 v77, 0\n"
      "v_mov_b32 v78, 0\n"
      "v_mov_b32 v79, 0\n"
      ".Lq_s1l:\n"
      "s_cmp_lt_u32 s52, s53\n"
      "s_cbranch_scc0 .Lq_s1n\n"
      "s_and_b32 s50, s52, 3\n"
      "s_lshl_b32 s51, s50, 17\n"
      "s_add_u32 s51, s51, s47\n"
      "buffer_load_dwordx4 v8, s[20:23], s51 offen offset:0 lds\n"
      "buffer_load_dwordx4 v9, s[20:23], s51 offen offset:1024 lds\n"
      "buffer_load_dwordx4 v10, s[20:23], s51 offen offset:2048 lds\n"
      "buffer_load_dwordx4 v11, s[20:23], s51 offen offset:3072 lds\n"
      "s_add_u32 m0, m0, 0x1000\n"
      "s_lshl_b32 s50, s50, 7\n"
      "s_add_u32 s50, s50, s46\n"
      "buffer_load_dwordx4 v12, s[4:7], s50 offen offset:0 lds\n"
      "buffer_load_dwordx4 v13, s[4:7], s50 offen offset:1024 lds\n"
      "buffer_load_dwordx4 v14, s[4:7], s50 offen offset:2048 lds\n"
      "buffer_load_dwordx4 v15, s[4:7], s50 offen offset:3072 lds\n"
      "s_add_u32 m0, m0, 0x1000\n"
      "s_add_u32 s52, s52, 1\n"
      "s_waitcnt vmcnt(8)\n"
      "s_branch .Lq_s1m\n"
      ".Lq_s1n:\n"
      "s_add_u32 s52, s52, 1\n"
      "s_waitcnt vmcnt(0)\n"
      ".Lq_s1m:\n"
      "ds_read_b64_tr_b16 v[16:17], v48 offset:0\n"
      "ds_read_b64_tr_b16 v[18:19], v48 offset:256\n"
      "ds_read_b128 v[24:27], v50 offset:4096\n"
      "ds_read_b64_tr_b16 v[20:21], v49 offset:0\n"
      "ds_read_b64_tr_b16 v[22:23], v49 offset:256\n"
      "ds_read_b128 v[28:31], v50 offset:6144\n"
      "ds_read_b64_tr_b16 v[32:33], v48 offset:2048\n"
      "ds_read_b64_tr_b16 v[34:35], v48 offset:2304\n"
      "ds_read_b128 v[40:43], v50 offset:5120\n"
      "ds_read_b64_tr_b16 v[36:37], v49 offset:2048\n"
      "ds_read_b64_tr_b16 v[38:39], v49 offset:2304\n"
      "ds_read_b128 v[44:47], v50 offset:7168\n"
      "v_add_u32 v48, 0x2000, v48\n"
      "v_add_u32 v49, 0x2000, v49\n"
      "v_add_u32 v50, 0x2000, v50\n"
      "s_waitcnt lgkmcnt(9)\n"
      "v_mfma_f32_16x16x32_bf16 v[64:67], v[16:19], v[24:27], v[64:67]\n"
      "s_waitcnt lgkmcnt(7)\n"
      "v_mfma_f32_16x16x32_bf16 v[68:71], v[20:23], v[24:27], v[68:71]\n"
      "s_waitcnt lgkmcnt(6)\n"
      "v_mfma_f32_16x16x32_bf16 v[72:75], v[16:19], v[28:31], v[72:75]\n"
      "v_mfma_f32_16x16x32_bf16 v[76:79], v[20:23], v[28:31], v[76:79]\n"
      "s_waitcnt lgkmcnt(3)\n"
      "v_mfma_f32_16x16x32_bf16 v[64:67], v[32:35], v[40:43], v[64:67]\n"
      "s_waitcnt lgkmcnt(1)\n"
      "v_mfma_f32_16x16x32_bf16 v[68:71], v[36:39], v[40:43], v[68:71]\n"
      "s_waitcnt lgkmcnt(0)\n"
      "v_mfma_f32_16x16x32_bf16 v[72:75], v[32:35], v[44:47], v[72:75]\n"
      "v_mfma_f32_16x16x32_bf16 v[76:79], v[36:39], v[44:47], v[76:79]\n"
      "s_cmp_le_u32 s52, s53\n"
      "s_cbranch_scc1 .Lq_s1l\n"
      "s_sub_u32 s50, s36, 1\n"
      "s_max_i32 s50, s50, 0\n"
      "s_lshl_b32 s50, s50, 11\n"
      "s_add_u32 s50, s50, 147456\n"
      "v_add_u32 v4, s50, v3\n"
      "s_lshl_b32 s52, s39, 1\n"
      "s_bfe_u32 s51, s38, 0x10002\n"
      "s_add_u32 s52, s52, s51\n"
      "s_lshl_b32 s52, s52, 1\n"
      "s_add_u32 s52, s52, s36\n"
      "s_lshl_b32 s52, s52, 10\n"
      "s_add_u32 s52, s52, 65536\n"
      "s_cmp_eq_u32 s36, 0\n"
      "s_cbranch_scc1 .Lq_s1w1\n"
      "ds_write_b128 v4, v[64:67]\n"
      "ds_write_b128 v4, v[68:71] offset:1024\n"
      "s_cmp_eq_u32 s36, 1\n"
      "s_cbranch_scc0 .Lq_s1w1\n"
      "v_add_u32 v4, 6144, v4\n"
      "v_mov_b32 v64, v72\n"
      "v_mov_b32 v65, v73\n"
      "v_mov_b32 v66, v74\n"
      "v_mov_b32 v67, v75\n"
      "v_mov_b32 v68, v76\n"
      "v_mov_b32 v69, v77\n"
      "v_mov_b32 v70, v78\n"
      "v_mov_b32 v71, v79\n"
      "s_branch .Lq_s1w2\n"
      ".Lq_s1w1:\n"
      "ds_write_b128 v4, v[72:75] offset:6144\n"
      "ds_write_b128 v4, v[76:79] offset:7168\n"
      ".Lq_s1w2:\n"
      "s_cmp_lt_u32 s36, 2\n"
      "s_waitcnt lgkmcnt(0)\n"
      "s_barrier\n"
      "s_cbranch_scc0 .Lq_s1e\n"
      "ds_read_b128 v[16:19], v4 offset:0\n"
      "ds_read_b128 v[20:23], v4 offset:1024\n"
      "ds_read_b128 v[24:27], v4 offset:2048\n"
      "ds_read_b128 v[28:31], v4 offset:3072\n"
      "ds_read_b128 v[32:35], v4 offset:4096\n"
      "ds_read_b128 v[36:39], v4 offset:5120\n"
      "v_mov_b32 v5, 0x7fff7fff\n"
      "s_waitcnt lgkmcnt(4)\n"
      "v_pk_add_f32 v[64:65], v[64:65], v[16:17]\n"
      "v_pk_add_f32 v[66:67], v[66:67], v[18:19]\n"
      "v_pk_add_f32 v[68:69], v[68:69], v[20:21]\n"
      "v_pk_add_f32 v[70:71], v[70:71], v[22:23]\n"
      "s_waitcnt lgkmcnt(2)\n"
      "v_pk_add_f32 v[64:65], v[64:65], v[24:25]\n"
      "v_pk_add_f32 v[66:67], v[66:67], v[26:27]\n"
      "v_pk_add_f32 v[68:69], v[68:69], v[28:29]\n"
      "v_pk_add_f32 v[70:71], v[70:71], v[30:31]\n"
      "s_waitcnt lgkmcnt(0)\n"
      "v_pk_add_f32 v[64:65], v[64:65], v[32:33]\n"
      "v_pk_add_f32 v[66:67], v[66:67], v[34:35]\n"
      "v_pk_add_f32 v[68:69], v[68:69], v[36:37]\n"
      "v_pk_add_f32 v[70:71], v[70:71], v[38:39]\n"
      "v_cvt_pk_bf16_f32 v12, v64, v65\n"
      "v_cvt_pk_bf16_f32 v13, v66, v67\n"
      "v_cvt_pk_bf16_f32 v14, v68, v69\n"
      "v_cvt_pk_bf16_f32 v15, v70, v71\n"
      "v_cmp_ne_u32 s[46:47], -1, v12\n"
      "v_cmp_ne_u32 s[48:49], -1, v13\n"
      "s_nop 0\n"
      "v_cndmask_b32 v12, v5, v12, s[46:47]\n"
      "v_cndmask_b32 v13, v5, v13, s[48:49]\n"
      "v_cmp_ne_u32 s[46:47], -1, v14\n"
      "v_cmp_ne_u32 s[48:49], -1, v15\n"
      "s_nop 0\n"
      "v_cndmask_b32 v14, v5, v14, s[46:47]\n"
      "v_cndmask_b32 v15, v5, v15, s[48:49]\n"
      "buffer_store_dwordx4 v[12:15], v3, s[32:35], s52 offen sc1\n"
      ".Lq_s1e:\n"
      "s_barrier\n"
      "s_cmp_lt_u32 s38, 28\n"
      "s_cbranch_scc1 .Lq_bmain\n"
      "s_cmp_eq_u32 s36, 0\n"
      "s_cbranch_scc0 .Lq_end0\n"
      "s_cmp_eq_u32 s2, 248\n"
      "s_cbranch_scc1 .Lq_retire\n"
      ".Lq_end0:\n"
      "s_endpgm\n"
      ".Lq_amain:\n"
      "s_setprio 1\n"
      "s_barrier\n"
      "s_lshl_b32 s63, s36, 15\n"
      "s_add_u32 s63, s63, 65536\n"
      "s_add_u32 s50, s36, 0\n"
      "s_and_b32 s50, s50, 3\n"
      "s_lshl_b32 s50, s50, 10\n"
      "v_add_u32 v176, s50, v3\n"
      "s_add_u32 s50, s36, 1\n"
      "s_and_b32 s50, s50, 3\n"
      "s_lshl_b32 s50, s50, 10\n"
      "v_add_u32 v177, s50, v3\n"
      "s_add_u32 s50, s36, 2\n"
      "s_and_b32 s50, s50, 3\n"
      "s_lshl_b32 s50, s50, 10\n"
      "v_add_u32 v178, s50, v3\n"
      "s_add_u32 s50, s36, 3\n"
      "s_and_b32 s50, s50, 3\n"
      "s_lshl_b32 s50, s50, 10\n"
      "v_add_u32 v179, s50, v3\n"
      "s_mul_i32 s50, s40, 19\n"
      "v_min_u32 v1, 18, v0\n"
      "v_add_u32 v1, s50, v1\n"
      "v_min_u32 v1, 255, v1\n"
      "v_lshlrev_b32 v1, 7, v1\n"
      "s_waitcnt vmcnt(14)\n"
      "buffer_load_dwordx4 v[56:59], v10, s[8:11], s47 offen nt\n"
      "buffer_load_dwordx4 v[60:63], v11, s[8:11], s47 offen nt\n"
      "v_lshlrev_b32 v164, 16, v144\n"
      "v_and_b32 v165, 0xffff0000, v144\n"
      "v_lshlrev_b32 v160, 16, v146\n"
      "v_and_b32 v161, 0xffff0000, v146\n"
      "v_lshlrev_b32 v166, 16, v145\n"
      "v_and_b32 v167, 0xffff0000, v145\n"
      "v_lshlrev_b32 v162, 16, v147\n"
      "v_and_b32 v163, 0xffff0000, v147\n"
      "v_pk_mul_f32 v[160:161], v[160:161], v[164:165]\n"
      "v_pk_mul_f32 v[162:163], v[162:163], v[166:167]\n"
      "v_cvt_f32_i32 v168, v16\n"
      "v_cvt_f32_i32 v169, v20\n"
      "v_cvt_f32_i32 v170, v17\n"
      "v_cvt_f32_i32 v171, v21\n"
      "v_cvt_f32_i32 v172, v18\n"
      "v_cvt_f32_i32 v173, v22\n"
      "v_cvt_f32_i32 v174, v19\n"
      "v_cvt_f32_i32 v175, v23\n"
      "v_pk_fma_f32 v[168:169], v[168:169], v[164:165], v[160:161] op_sel:[0,0,0] op_sel_hi:[1,0,0] neg_lo:[0,0,1] neg_hi:[0,0,1]\n"
      "v_pk_fma_f32 v[170:171], v[170:171], v[164:165], v[160:161] op_sel:[0,1,1] op_sel_hi:[1,1,1] neg_lo:[0,0,1] neg_hi:[0,0,1]\n"
      "v_pk_fma_f32 v[172:173], v[172:173], v[166:167], v[162:163] op_sel:[0,0,0] op_sel_hi:[1,0,0] neg_lo:[0,0,1] neg_hi:[0,0,1]\n"
      "v_pk_fma_f32 v[174:175], v[174:175], v[166:167], v[162:163] op_sel:[0,1,1] op_sel_hi:[1,1,1] neg_lo:[0,0,1] neg_hi:[0,0,1]\n"
      "v_cvt_pk_bf16_f32 v80, v168, v169\n"
      "v_cvt_pk_bf16_f32 v88, v170, v171\n"
      "v_cvt_pk_bf16_f32 v84, v172, v173\n"
      "v_cvt_pk_bf16_f32 v92, v174, v175\n"
      "v_mov_b32 v4, v80\n"
      "v_mov_b32 v5, v84\n"
      "v_mov_b32_dpp v80, v88 row_ror:8 row_mask:0xf bank_mask:0xc\n"
      "v_mov_b32_dpp v84, v92 row_ror:8 row_mask:0xf bank_mask:0xc\n"
      "v_mov_b32_dpp v88, v4 row_ror:8 row_mask:0xf bank_mask:0x3\n"
      "v_mov_b32_dpp v92, v5 row_ror:8 row_mask:0xf bank_mask:0x3\n"
      "s_waitcnt vmcnt(14)\n"
      "buffer_load_dwordx4 v[64:67], v12, s[8:11], s47 offen nt\n"
      "buffer_load_dwordx4 v[68:71], v13, s[8:11], s47 offen nt\n"
      "v_cvt_f32_i32 v168, v24\n"
      "v_cvt_f32_i32 v169, v28\n"
      "v_cvt_f32_i32 v170, v25\n"
      "v_cvt_f32_i32 v171, v29\n"
      "v_cvt_f32_i32 v172, v26\n"
      "v_cvt_f32_i32 v173, v30\n"
      "v_cvt_f32_i32 v174, v27\n"
      "v_cvt_f32_i32 v175, v31\n"
      "v_pk_fma_f32 v[168:169], v[168:169], v[164:165], v[160:161] op_sel:[0,0,0] op_sel_hi:[1,0,0] neg_lo:[0,0,1] neg_hi:[0,0,1]\n"
      "v_pk_fma_f32 v[170:171], v[170:171], v[164:165], v[160:161] op_sel:[0,1,1] op_sel_hi:[1,1,1] neg_lo:[0,0,1] neg_hi:[0,0,1]\n"
      "v_pk_fma_f32 v[172:173], v[172:173], v[166:167], v[162:163] op_sel:[0,0,0] op_sel_hi:[1,0,0] neg_lo:[0,0,1] neg_hi:[0,0,1]\n"
      "v_pk_fma_f32 v[174:175], v[174:175], v[166:167], v[162:163] op_sel:[0,1,1] op_sel_hi:[1,1,1] neg_lo:[0,0,1] neg_hi:[0,0,1]\n"
      "v_cvt_pk_bf16_f32 v81, v168, v169\n"
      "v_cvt_pk_bf16_f32 v89, v170, v171\n"
      "v_cvt_pk_bf16_f32 v85, v172, v173\n"
      "v_cvt_pk_bf16_f32 v93, v174, v175\n"
      "v_mov_b32 v4, v81\n"
      "v_mov_b32 v5, v85\n"
      "v_mov_b32_dpp v81, v89 row_ror:8 row_mask:0xf bank_mask:0xc\n"
      "v_mov_b32_dpp v85, v93 row_ror:8 row_mask:0xf bank_mask:0xc\n"
      "v_mov_b32_dpp v89, v4 row_ror:8 row_mask:0xf bank_mask:0x3\n"
      "v_mov_b32_dpp v93, v5 row_ror:8 row_mask:0xf bank_mask:0x3\n"
      "s_waitcnt vmcnt(14)\n"
      "buffer_load_dwordx4 v[72:75], v14, s[8:11], s47 offen nt\n"
      "buffer_load_dwordx4 v[76:79], v15, s[8:11], s47 offen nt\n"
      "v_cvt_f32_i32 v168, v32\n"
      "v_cvt_f32_i32 v169, v36\n"
      "v_cvt_f32_i32 v170, v33\n"
      "v_cvt_f32_i32 v171, v37\n"
      "v_cvt_f32_i32 v172, v34\n"
      "v_cvt_f32_i32 v173, v38\n"
      "v_cvt_f32_i32 v174, v35\n"
      "v_cvt_f32_i32 v175, v39\n"
      "v_pk_fma_f32 v[168:169], v[168:169], v[164:165], v[160:161] op_sel:[0,0,0] op_sel_hi:[1,0,0] neg_lo:[0,0,1] neg_hi:[0,0,1]\n"
      "v_pk_fma_f32 v[170:171], v[170:171], v[164:165], v[160:161] op_sel:[0,1,1] op_sel_hi:[1,1,1] neg_lo:[0,0,1] neg_hi:[0,0,1]\n"
      "v_pk_fma_f32 v[172:173], v[172:173], v[166:167], v[162:163] op_sel:[0,0,0] op_sel_hi:[1,0,0] neg_lo:[0,0,1] neg_hi:[0,0,1]\n"
      "v_pk_fma_f32 v[174:175], v[174:175], v[166:167], v[162:163] op_sel:[0,1,1] op_sel_hi:[1,1,1] neg_lo:[0,0,1] neg_hi:[0,0,1]\n"
      "v_cvt_pk_bf16_f32 v82, v168, v169\n"
      "v_cvt_pk_bf16_f32 v90, v170, v171\n"
      "v_cvt_pk_bf16_f32 v86, v172, v173\n"
      "v_cvt_pk_bf16_f32 v94, v174, v175\n"
      "v_mov_b32 v4, v82\n"
      "v_mov_b32 v5, v86\n"
      "v_mov_b32_dpp v82, v90 row_ror:8 row_mask:0xf bank_mask:0xc\n"
      "v_mov_b32_dpp v86, v94 row_ror:8 row_mask:0xf bank_mask:0xc\n"
      "v_mov_b32_dpp v90, v4 row_ror:8 row_mask:0xf bank_mask:0x3\n"
      "v_mov_b32_dpp v94, v5 row_ror:8 row_mask:0xf bank_mask:0x3\n"
      "s_waitcnt vmcnt(14)\n"
      "buffer_load_dwordx4 v[16:19], v8, s[8:11], s48 offen nt\n"
      "buffer_load_dwordx4 v[20:23], v9, s[8:11], s48 offen nt\n"
      "v_cvt_f32_i32 v168, v40\n"
      "v_cvt_f32_i32 v169, v44\n"
      "v_cvt_f32_i32 v170, v41\n"
      "v_cvt_f32_i32 v171, v45\n"
      "v_cvt_f32_i32 v172, v42\n"
      "v_cvt_f32_i32 v173, v46\n"
      "v_cvt_f32_i32 v174, v43\n"
      "v_cvt_f32_i32 v175, v47\n"
      "v_pk_fma_f32 v[168:169], v[168:169], v[164:165], v[160:161] op_sel:[0,0,0] op_sel_hi:[1,0,0] neg_lo:[0,0,1] neg_hi:[0,0,1]\n"
      "v_pk_fma_f32 v[170:171], v[170:171], v[164:165], v[160:161] op_sel:[0,1,1] op_sel_hi:[1,1,1] neg_lo:[0,0,1] neg_hi:[0,0,1]\n"
      "v_pk_fma_f32 v[172:173], v[172:173], v[166:167], v[162:163] op_sel:[0,0,0] op_sel_hi:[1,0,0] neg_lo:[0,0,1] neg_hi:[0,0,1]\n"
      "v_pk_fma_f32 v[174:175], v[174:175], v[166:167], v[162:163] op_sel:[0,1,1] op_sel_hi:[1,1,1] neg_lo:[0,0,1] neg_hi:[0,0,1]\n"
      "v_cvt_pk_bf16_f32 v83, v168, v169\n"
      "v_cvt_pk_bf16_f32 v91, v170, v171\n"
      "v_cvt_pk_bf16_f32 v87, v172, v173\n"
      "v_cvt_pk_bf16_f32 v95, v174, v175\n"
      "v_mov_b32 v4, v83\n"
      "v_mov_b32 v5, v87\n"
      "v_mov_b32_dpp v83, v91 row_ror:8 row_mask:0xf bank_mask:0xc\n"
      "v_mov_b32_dpp v87, v95 row_ror:8 row_mask:0xf bank_mask:0xc\n"
      "v_mov_b32_dpp v91, v4 row_ror:8 row_mask:0xf bank_mask:0x3\n"
      "v_mov_b32_dpp v95, v5 row_ror:8 row_mask:0xf bank_mask:0x3\n"
      "s_waitcnt vmcnt(8)\n"
      "buffer_load_dwordx4 v[24:27], v10, s[8:11], s48 offen nt\n"
      "buffer_load_dwordx4 v[28:31], v11, s[8:11], s48 offen nt\n"
      "v_lshlrev_b32 v164, 16, v148\n"
      "v_and_b32 v165, 0xffff0000, v148\n"
      "v_lshlrev_b32 v160, 16, v150\n"
      "v_and_b32 v161, 0xffff0000, v150\n"
      "v_lshlrev_b32 v166, 16, v149\n"
      "v_and_b32 v167, 0xffff0000, v149\n"
      "v_lshlrev_b32 v162, 16, v151\n"
      "v_and_b32 v163, 0xffff0000, v151\n"
      "v_pk_mul_f32 v[160:161], v[160:161], v[164:165]\n"
      "v_pk_mul_f32 v[162:163], v[162:163], v[166:167]\n"
      "v_cvt_f32_i32 v168, v48\n"
      "v_cvt_f32_i32 v169, v52\n"
      "v_cvt_f32_i32 v170, v49\n"
      "v_cvt_f32_i32 v171, v53\n"
      "v_cvt_f32_i32 v172, v50\n"
      "v_cvt_f32_i32 v173, v54\n"
      "v_cvt_f32_i32 v174, v51\n"
      "v_cvt_f32_i32 v175, v55\n"
      "v_pk_fma_f32 v[168:169], v[168:169], v[164:165], v[160:161] op_sel:[0,0,0] op_sel_hi:[1,0,0] neg_lo:[0,0,1] neg_hi:[0,0,1]\n"
      "v_pk_fma_f32 v[170:171], v[170:171], v[164:165], v[160:161] op_sel:[0,1,1] op_sel_hi:[1,1,1] neg_lo:[0,0,1] neg_hi:[0,0,1]\n"
      "v_pk_fma_f32 v[172:173], v[172:173], v[166:167], v[162:163] op_sel:[0,0,0] op_sel_hi:[1,0,0] neg_lo:[0,0,1] neg_hi:[0,0,1]\n"
      "v_pk_fma_f32 v[174:175], v[174:175], v[166:167], v[162:163] op_sel:[0,1,1] op_sel_hi:[1,1,1] neg_lo:[0,0,1] neg_hi:[0,0,1]\n"
      "v_cvt_pk_bf16_f32 v96, v168, v169\n"
      "v_cvt_pk_bf16_f32 v104, v170, v171\n"
      "v_cvt_pk_bf16_f32 v100, v172, v173\n"
      "v_cvt_pk_bf16_f32 v108, v174, v175\n"
      "v_mov_b32 v4, v96\n"
      "v_mov_b32 v5, v100\n"
      "v_mov_b32_dpp v96, v104 row_ror:8 row_mask:0xf bank_mask:0xc\n"
      "v_mov_b32_dpp v100, v108 row_ror:8 row_mask:0xf bank_mask:0xc\n"
      "v_mov_b32_dpp v104, v4 row_ror:8 row_mask:0xf bank_mask:0x3\n"
      "v_mov_b32_dpp v108, v5 row_ror:8 row_mask:0xf bank_mask:0x3\n"
      "s_waitcnt vmcnt(8)\n"
      "buffer_load_dwordx4 v[32:35], v12, s[8:11], s48 offen nt\n"
      "buffer_load_dwordx4 v[36:39], v13, s[8:11], s48 offen nt\n"
      "v_mov_b32 v2, 6\n"
      "ds_write_b32 v7, v2\n"
      "v_cvt_f32_i32 v168, v56\n"
      "v_cvt_f32_i32 v169, v60\n"
      "v_cvt_f32_i32 v170, v57\n"
      "v_cvt_f32_i32 v171, v61\n"
      "v_cvt_f32_i32 v172, v58\n"
      "v_cvt_f32_i32 v173, v62\n"
      "v_cvt_f32_i32 v174, v59\n"
      "v_cvt_f32_i32 v175, v63\n"
      "v_pk_fma_f32 v[168:169], v[168:169], v[164:165], v[160:161] op_sel:[0,0,0] op_sel_hi:[1,0,0] neg_lo:[0,0,1] neg_hi:[0,0,1]\n"
      "v_pk_fma_f32 v[170:171], v[170:171], v[164:165], v[160:161] op_sel:[0,1,1] op_sel_hi:[1,1,1] neg_lo:[0,0,1] neg_hi:[0,0,1]\n"
      "v_pk_fma_f32 v[172:173], v[172:173], v[166:167], v[162:163] op_sel:[0,0,0] op_sel_hi:[1,0,0] neg_lo:[0,0,1] neg_hi:[0,0,1]\n"
      "v_pk_fma_f32 v[174:175], v[174:175], v[166:167], v[162:163] op_sel:[0,1,1] op_sel_hi:[1,1,1] neg_lo:[0,0,1] neg_hi:[0,0,1]\n"
      "v_cvt_pk_bf16_f32 v97, v168, v169\n"
      "v_cvt_pk_bf16_f32 v105, v170, v171\n"
      "v_cvt_pk_bf16_f32 v101, v172, v173\n"
      "v_cvt_pk_bf16_f32 v109, v174, v175\n"
      "v_mov_b32 v4, v97\n"
      "v_mov_b32 v5, v101\n"
      "v_mov_b32_dpp v97, v105 row_ror:8 row_mask:0xf bank_mask:0xc\n"
      "v_mov_b32_dpp v101, v109 row_ror:8 row_mask:0xf bank_mask:0xc\n"
      "v_mov_b32_dpp v105, v4 row_ror:8 row_mask:0xf bank_mask:0x3\n"
      "v_mov_b32_dpp v109, v5 row_ror:8 row_mask:0xf bank_mask:0x3\n"
      "s_waitcnt vmcnt(8)\n"
      "buffer_load_dwordx4 v[40:43], v14, s[8:11], s48 offen nt\n"
      "buffer_load_dwordx4 v[44:47], v15, s[8:11], s48 offen nt\n"
      "v_cvt_f32_i32 v168, v64\n"
      "v_cvt_f32_i32 v169, v68\n"
      "v_cvt_f32_i32 v170, v65\n"
      "v_cvt_f32_i32 v171, v69\n"
      "v_cvt_f32_i32 v172, v66\n"
      "v_cvt_f32_i32 v173, v70\n"
      "v_cvt_f32_i32 v174, v67\n"
      "v_cvt_f32_i32 v175, v71\n"
      "v_pk_fma_f32 v[168:169], v[168:169], v[164:165], v[160:161] op_sel:[0,0,0] op_sel_hi:[1,0,0] neg_lo:[0,0,1] neg_hi:[0,0,1]\n"
      "v_pk_fma_f32 v[170:171], v[170:171], v[164:165], v[160:161] op_sel:[0,1,1] op_sel_hi:[1,1,1] neg_lo:[0,0,1] neg_hi:[0,0,1]\n"
      "v_pk_fma_f32 v[172:173], v[172:173], v[166:167], v[162:163] op_sel:[0,0,0] op_sel_hi:[1,0,0] neg_lo:[0,0,1] neg_hi:[0,0,1]\n"
      "v_pk_fma_f32 v[174:175], v[174:175], v[166:167], v[162:163] op_sel:[0,1,1] op_sel_hi:[1,1,1] neg_lo:[0,0,1] neg_hi:[0,0,1]\n"
      "v_cvt_pk_bf16_f32 v98, v168, v169\n"
      "v_cvt_pk_bf16_f32 v106, v170, v171\n"
      "v_cvt_pk_bf16_f32 v102, v172, v173\n"
      "v_cvt_pk_bf16_f32 v110, v174, v175\n"
      "v_mov_b32 v4, v98\n"
      "v_mov_b32 v5, v102\n"
      "v_mov_b32_dpp v98, v106 row_ror:8 row_mask:0xf bank_mask:0xc\n"
      "v_mov_b32_dpp v102, v110 row_ror:8 row_mask:0xf bank_mask:0xc\n"
      "v_mov_b32_dpp v106, v4 row_ror:8 row_mask:0xf bank_mask:0x3\n"
      "v_mov_b32_dpp v110, v5 row_ror:8 row_mask:0xf bank_mask:0x3\n"
      "s_waitcnt vmcnt(8)\n"
      "buffer_load_dwordx4 v[48:51], v8, s[8:11], s49 offen nt\n"
      "buffer_load_dwordx4 v[52:55], v9, s[8:11], s49 offen nt\n"
      "v_cvt_f32_i32 v168, v72\n"
      "v_cvt_f32_i32 v169, v76\n"
      "v_cvt_f32_i32 v170, v73\n"
      "v_cvt_f32_i32 v171, v77\n"
      "v_cvt_f32_i32 v172, v74\n"
      "v_cvt_f32_i32 v173, v78\n"
      "v_cvt_f32_i32 v174, v75\n"
      "v_cvt_f32_i32 v175, v79\n"
      "v_pk_fma_f32 v[168:169], v[168:169], v[164:165], v[160:161] op_sel:[0,0,0] op_sel_hi:[1,0,0] neg_lo:[0,0,1] neg_hi:[0,0,1]\n"
      "v_pk_fma_f32 v[170:171], v[170:171], v[164:165], v[160:161] op_sel:[0,1,1] op_sel_hi:[1,1,1] neg_lo:[0,0,1] neg_hi:[0,0,1]\n"
      "v_pk_fma_f32 v[172:173], v[172:173], v[166:167], v[162:163] op_sel:[0,0,0] op_sel_hi:[1,0,0] neg_lo:[0,0,1] neg_hi:[0,0,1]\n"
      "v_pk_fma_f32 v[174:175], v[174:175], v[166:167], v[162:163] op_sel:[0,1,1] op_sel_hi:[1,1,1] neg_lo:[0,0,1] neg_hi:[0,0,1]\n"
      "v_cvt_pk_bf16_f32 v99, v168, v169\n"
      "v_cvt_pk_bf16_f32 v107, v170, v171\n"
      "v_cvt_pk_bf16_f32 v103, v172, v173\n"
      "v_cvt_pk_bf16_f32 v111, v174, v175\n"
      "v_mov_b32 v4, v99\n"
      "v_mov_b32 v5, v103\n"
      "v_mov_b32_dpp v99, v107 row_ror:8 row_mask:0xf bank_mask:0xc\n"
      "v_mov_b32_dpp v103, v111 row_ror:8 row_mask:0xf bank_mask:0xc\n"
      "v_mov_b32_dpp v107, v4 row_ror:8 row_mask:0xf bank_mask:0x3\n"
      "v_mov_b32_dpp v111, v5 row_ror:8 row_mask:0xf bank_mask:0x3\n"
      "s_waitcnt vmcnt(8)\n"
      "buffer_load_dwordx4 v[56:59], v10, s[8:11], s49 offen nt\n"
      "buffer_load_dwordx4 v[60:63], v11, s[8:11], s49 offen nt\n"
      "v_lshlrev_b32 v164, 16, v152\n"
      "v_and_b32 v165, 0xffff0000, v152\n"
      "v_lshlrev_b32 v160, 16, v154\n"
      "v_and_b32 v161, 0xffff0000, v154\n"
      "v_lshlrev_b32 v166, 16, v153\n"
      "v_and_b32 v167, 0xffff0000, v153\n"
      "v_lshlrev_b32 v162, 16, v155\n"
      "v_and_b32 v163, 0xffff0000, v155\n"
      "v_pk_mul_f32 v[160:161], v[160:161], v[164:165]\n"
      "v_pk_mul_f32 v[162:163], v[162:163], v[166:167]\n"
      "v_cvt_f32_i32 v168, v16\n"
      "v_cvt_f32_i32 v169, v20\n"
      "v_cvt_f32_i32 v170, v17\n"
      "v_cvt_f32_i32 v171, v21\n"
      "v_cvt_f32_i32 v172, v18\n"
      "v_cvt_f32_i32 v173, v22\n"
      "v_cvt_f32_i32 v174, v19\n"
      "v_cvt_f32_i32 v175, v23\n"
      "v_pk_fma_f32 v[168:169], v[168:169], v[164:165], v[160:161] op_sel:[0,0,0] op_sel_hi:[1,0,0] neg_lo:[0,0,1] neg_hi:[0,0,1]\n"
      "v_pk_fma_f32 v[170:171], v[170:171], v[164:165], v[160:161] op_sel:[0,1,1] op_sel_hi:[1,1,1] neg_lo:[0,0,1] neg_hi:[0,0,1]\n"
      "v_pk_fma_f32 v[172:173], v[172:173], v[166:167], v[162:163] op_sel:[0,0,0] op_sel_hi:[1,0,0] neg_lo:[0,0,1] neg_hi:[0,0,1]\n"
      "v_pk_fma_f32 v[174:175], v[174:175], v[166:167], v[162:163] op_sel:[0,1,1] op_sel_hi:[1,1,1] neg_lo:[0,0,1] neg_hi:[0,0,1]\n"
      "v_cvt_pk_bf16_f32 v112, v168, v169\n"
      "v_cvt_pk_bf16_f32 v120, v170, v171\n"
      "v_cvt_pk_bf16_f32 v116, v172, v173\n"
      "v_cvt_pk_bf16_f32 v124, v174, v175\n"
      "v_mov_b32 v4, v112\n"
      "v_mov_b32 v5, v116\n"
      "v_mov_b32_dpp v112, v120 row_ror:8 row_mask:0xf bank_mask:0xc\n"
      "v_mov_b32_dpp v116, v124 row_ror:8 row_mask:0xf bank_mask:0xc\n"
      "v_mov_b32_dpp v120, v4 row_ror:8 row_mask:0xf bank_mask:0x3\n"
      "v_mov_b32_dpp v124, v5 row_ror:8 row_mask:0xf bank_mask:0x3\n"
      "s_waitcnt vmcnt(8)\n"
      "buffer_load_dword v211, v1, s[32:35], s63 offen sc1\n"
      "buffer_load_dwordx4 v[64:67], v12, s[8:11], s49 offen nt\n"
      "buffer_load_dwordx4 v[68:71], v13, s[8:11], s49 offen nt\n"
      "v_cvt_f32_i32 v168, v24\n"
      "v_cvt_f32_i32 v169, v28\n"
      "v_cvt_f32_i32 v170, v25\n"
      "v_cvt_f32_i32 v171, v29\n"
      "v_cvt_f32_i32 v172, v26\n"
      "v_cvt_f32_i32 v173, v30\n"
      "v_cvt_f32_i32 v174, v27\n"
      "v_cvt_f32_i32 v175, v31\n"
      "v_pk_fma_f32 v[168:169], v[168:169], v[164:165], v[160:161] op_sel:[0,0,0] op_sel_hi:[1,0,0] neg_lo:[0,0,1] neg_hi:[0,0,1]\n"
      "v_pk_fma_f32 v[170:171], v[170:171], v[164:165], v[160:161] op_sel:[0,1,1] op_sel_hi:[1,1,1] neg_lo:[0,0,1] neg_hi:[0,0,1]\n"
      "v_pk_fma_f32 v[172:173], v[172:173], v[166:167], v[162:163] op_sel:[0,0,0] op_sel_hi:[1,0,0] neg_lo:[0,0,1] neg_hi:[0,0,1]\n"
      "v_pk_fma_f32 v[174:175], v[174:175], v[166:167], v[162:163] op_sel:[0,1,1] op_sel_hi:[1,1,1] neg_lo:[0,0,1] neg_hi:[0,0,1]\n"
      "v_cvt_pk_bf16_f32 v113, v168, v169\n"
      "v_cvt_pk_bf16_f32 v121, v170, v171\n"
      "v_cvt_pk_bf16_f32 v117, v172, v173\n"
      "v_cvt_pk_bf16_f32 v125, v174, v175\n"
      "v_mov_b32 v4, v113\n"
      "v_mov_b32 v5, v117\n"
      "v_mov_b32_dpp v113, v121 row_ror:8 row_mask:0xf bank_mask:0xc\n"
      "v_mov_b32_dpp v117, v125 row_ror:8 row_mask:0xf bank_mask:0xc\n"
      "v_mov_b32_dpp v121, v4 row_ror:8 row_mask:0xf bank_mask:0x3\n"
      "v_mov_b32_dpp v125, v5 row_ror:8 row_mask:0xf bank_mask:0x3\n"
      "s_waitcnt vmcnt(9)\n"
      "buffer_load_dwordx4 v[72:75], v14, s[8:11], s49 offen nt\n"
      "buffer_load_dwordx4 v[76:79], v15, s[8:11], s49 offen nt\n"
      "v_cvt_f32_i32 v168, v32\n"
      "v_cvt_f32_i32 v169, v36\n"
      "v_cvt_f32_i32 v170, v33\n"
      "v_cvt_f32_i32 v171, v37\n"
      "v_cvt_f32_i32 v172, v34\n"
      "v_cvt_f32_i32 v173, v38\n"
      "v_cvt_f32_i32 v174, v35\n"
      "v_cvt_f32_i32 v175, v39\n"
      "v_pk_fma_f32 v[168:169], v[168:169], v[164:165], v[160:161] op_sel:[0,0,0] op_sel_hi:[1,0,0] neg_lo:[0,0,1] neg_hi:[0,0,1]\n"
      "v_pk_fma_f32 v[170:171], v[170:171], v[164:165], v[160:161] op_sel:[0,1,1] op_sel_hi:[1,1,1] neg_lo:[0,0,1] neg_hi:[0,0,1]\n"
      "v_pk_fma_f32 v[172:173], v[172:173], v[166:167], v[162:163] op_sel:[0,0,0] op_sel_hi:[1,0,0] neg_lo:[0,0,1] neg_hi:[0,0,1]\n"
      "v_pk_fma_f32 v[174:175], v[174:175], v[166:167], v[162:163] op_sel:[0,1,1] op_sel_hi:[1,1,1] neg_lo:[0,0,1] neg_hi:[0,0,1]\n"
      "v_cvt_pk_bf16_f32 v114, v168, v169\n"
      "v_cvt_pk_bf16_f32 v122, v170, v171\n"
      "v_cvt_pk_bf16_f32 v118, v172, v173\n"
      "v_cvt_pk_bf16_f32 v126, v174, v175\n"
      "v_mov_b32 v4, v114\n"
      "v_mov_b32 v5, v118\n"
      "v_mov_b32_dpp v114, v122 row_ror:8 row_mask:0xf bank_mask:0xc\n"
      "v_mov_b32_dpp v118, v126 row_ror:8 row_mask:0xf bank_mask:0xc\n"
      "v_mov_b32_dpp v122, v4 row_ror:8 row_mask:0xf bank_mask:0x3\n"
      "v_mov_b32_dpp v126, v5 row_ror:8 row_mask:0xf bank_mask:0x3\n"
      "s_waitcnt vmcnt(9)\n"
      "s_mov_b32 s54, 262144\n"
      ".Lq_spin3:\n"
      "ds_read_b32 v2, v6 offset:1280\n"
      "s_sub_u32 s54, s54, 1\n"
      "s_cbranch_scc1 .Lq_spe4\n"
      "s_waitcnt lgkmcnt(0)\n"
      "v_readfirstlane_b32 s55, v2\n"
      "s_cmp_ge_u32 s55, 1\n"
      "s_cbranch_scc0 .Lq_spin3\n"
      ".Lq_spe4:\n"
      "s_add_u32 s53, s63, 0\n"
      "buffer_load_dwordx4 v[16:19], v176, s[32:35], s53 offen sc1\n"
      "buffer_load_dwordx4 v[20:23], v177, s[32:35], s53 offen sc1\n"
      "buffer_load_dwordx4 v[24:27], v178, s[32:35], s53 offen sc1\n"
      "buffer_load_dwordx4 v[28:31], v179, s[32:35], s53 offen sc1\n"
      "v_cvt_f32_i32 v168, v40\n"
      "v_cvt_f32_i32 v169, v44\n"
      "v_cvt_f32_i32 v170, v41\n"
      "v_cvt_f32_i32 v171, v45\n"
      "v_cvt_f32_i32 v172, v42\n"
      "v_cvt_f32_i32 v173, v46\n"
      "v_cvt_f32_i32 v174, v43\n"
      "v_cvt_f32_i32 v175, v47\n"
      "v_pk_fma_f32 v[168:169], v[168:169], v[164:165], v[160:161] op_sel:[0,0,0] op_sel_hi:[1,0,0] neg_lo:[0,0,1] neg_hi:[0,0,1]\n"
      "v_pk_fma_f32 v[170:171], v[170:171], v[164:165], v[160:161] op_sel:[0,1,1] op_sel_hi:[1,1,1] neg_lo:[0,0,1] neg_hi:[0,0,1]\n"
      "v_pk_fma_f32 v[172:173], v[172:173], v[166:167], v[162:163] op_sel:[0,0,0] op_sel_hi:[1,0,0] neg_lo:[0,0,1] neg_hi:[0,0,1]\n"
      "v_pk_fma_f32 v[174:175], v[174:175], v[166:167], v[162:163] op_sel:[0,1,1] op_sel_hi:[1,1,1] neg_lo:[0,0,1] neg_hi:[0,0,1]\n"
      "v_cvt_pk_bf16_f32 v115, v168, v169\n"
      "v_cvt_pk_bf16_f32 v123, v170, v171\n"
      "v_cvt_pk_bf16_f32 v119, v172, v173\n"
      "v_cvt_pk_bf16_f32 v127, v174, v175\n"
      "v_mov_b32 v4, v115\n"
      "v_mov_b32 v5, v119\n"
      "v_mov_b32_dpp v115, v123 row_ror:8 row_mask:0xf bank_mask:0xc\n"
      "v_mov_b32_dpp v119, v127 row_ror:8 row_mask:0xf bank_mask:0xc\n"
      "v_mov_b32_dpp v123, v4 row_ror:8 row_mask:0xf bank_mask:0x3\n"
      "v_mov_b32_dpp v127, v5 row_ror:8 row_mask:0xf bank_mask:0x3\n"
      "s_waitcnt vmcnt(11)\n"
      "s_add_u32 s53, s63, 4096\n"
      "buffer_load_dwordx4 v[32:35], v176, s[32:35], s53 offen sc1\n"
      "buffer_load_dwordx4 v[36:39], v177, s[32:35], s53 offen sc1\n"
      "v_lshlrev_b32 v164, 16, v156\n"
      "v_and_b32 v165, 0xffff0000, v156\n"
      "v_lshlrev_b32 v160, 16, v158\n"
      "v_and_b32 v161, 0xffff0000, v158\n"
      "v_lshlrev_b32 v166, 16, v157\n"
      "v_and_b32 v167, 0xffff0000, v157\n"
      "v_lshlrev_b32 v162, 16, v159\n"
      "v_and_b32 v163, 0xffff0000, v159\n"
      "v_pk_mul_f32 v[160:161], v[160:161], v[164:165]\n"
      "v_pk_mul_f32 v[162:163], v[162:163], v[166:167]\n"
      "v_cvt_f32_i32 v168, v48\n"
      "v_cvt_f32_i32 v169, v52\n"
      "v_cvt_f32_i32 v170, v49\n"
      "v_cvt_f32_i32 v171, v53\n"
      "v_cvt_f32_i32 v172, v50\n"
      "v_cvt_f32_i32 v173, v54\n"
      "v_cvt_f32_i32 v174, v51\n"
      "v_cvt_f32_i32 v175, v55\n"
      "v_pk_fma_f32 v[168:169], v[168:169], v[164:165], v[160:161] op_sel:[0,0,0] op_sel_hi:[1,0,0] neg_lo:[0,0,1] neg_hi:[0,0,1]\n"
      "v_pk_fma_f32 v[170:171], v[170:171], v[164:165], v[160:161] op_sel:[0,1,1] op_sel_hi:[1,1,1] neg_lo:[0,0,1] neg_hi:[0,0,1]\n"
      "v_pk_fma_f32 v[172:173], v[172:173], v[166:167], v[162:163] op_sel:[0,0,0] op_sel_hi:[1,0,0] neg_lo:[0,0,1] neg_hi:[0,0,1]\n"
      "v_pk_fma_f32 v[174:175], v[174:175], v[166:167], v[162:163] op_sel:[0,1,1] op_sel_hi:[1,1,1] neg_lo:[0,0,1] neg_hi:[0,0,1]\n"
      "v_cvt_pk_bf16_f32 v128, v168, v169\n"
      "v_cvt_pk_bf16_f32 v136, v170, v171\n"
      "v_cvt_pk_bf16_f32 v132, v172, v173\n"
      "v_cvt_pk_bf16_f32 v140, v174, v175\n"
      "v_mov_b32 v4, v128\n"
      "v_mov_b32 v5, v132\n"
      "v_mov_b32_dpp v128, v136 row_ror:8 row_mask:0xf bank_mask:0xc\n"
      "v_mov_b32_dpp v132, v140 row_ror:8 row_mask:0xf bank_mask:0xc\n"
      "v_mov_b32_dpp v136, v4 row_ror:8 row_mask:0xf bank_mask:0x3\n"
      "v_mov_b32_dpp v140, v5 row_ror:8 row_mask:0xf bank_mask:0x3\n"
      "s_waitcnt vmcnt(11)\n"
      "buffer_load_dwordx4 v[40:43], v178, s[32:35], s53 offen sc1\n"
      "buffer_load_dwordx4 v[44:47], v179, s[32:35], s53 offen sc1\n"
      "v_cvt_f32_i32 v168, v56\n"
      "v_cvt_f32_i32 v169, v60\n"
      "v_cvt_f32_i32 v170, v57\n"
      "v_cvt_f32_i32 v171, v61\n"
      "v_cvt_f32_i32 v172, v58\n"
      "v_cvt_f32_i32 v173, v62\n"
      "v_cvt_f32_i32 v174, v59\n"
      "v_cvt_f32_i32 v175, v63\n"
      "v_pk_fma_f32 v[168:169], v[168:169], v[164:165], v[160:161] op_sel:[0,0,0] op_sel_hi:[1,0,0] neg_lo:[0,0,1] neg_hi:[0,0,1]\n"
      "v_pk_fma_f32 v[170:171], v[170:171], v[164:165], v[160:161] op_sel:[0,1,1] op_sel_hi:[1,1,1] neg_lo:[0,0,1] neg_hi:[0,0,1]\n"
      "v_pk_fma_f32 v[172:173], v[172:173], v[166:167], v[162:163] op_sel:[0,0,0] op_sel_hi:[1,0,0] neg_lo:[0,0,1] neg_hi:[0,0,1]\n"
      "v_pk_fma_f32 v[174:175], v[174:175], v[166:167], v[162:163] op_sel:[0,1,1] op_sel_hi:[1,1,1] neg_lo:[0,0,1] neg_hi:[0,0,1]\n"
      "v_cvt_pk_bf16_f32 v129, v168, v169\n"
      "v_cvt_pk_bf16_f32 v137, v170, v171\n"
      "v_cvt_pk_bf16_f32 v133, v172, v173\n"
      "v_cvt_pk_bf16_f32 v141, v174, v175\n"
      "v_mov_b32 v4, v129\n"
      "v_mov_b32 v5, v133\n"
      "v_mov_b32_dpp v129, v137 row_ror:8 row_mask:0xf bank_mask:0xc\n"
      "v_mov_b32_dpp v133, v141 row_ror:8 row_mask:0xf bank_mask:0xc\n"
      "v_mov_b32_dpp v137, v4 row_ror:8 row_mask:0xf bank_mask:0x3\n"
      "v_mov_b32_dpp v141, v5 row_ror:8 row_mask:0xf bank_mask:0x3\n"
      "s_waitcnt vmcnt(10)\n"
      "s_add_u32 s53, s63, 8192\n"
      "buffer_load_dwordx4 v[48:51], v176, s[32:35], s53 offen sc1\n"
      "buffer_load_dwordx4 v[52:55], v177, s[32:35], s53 offen sc1\n"
      "v_cvt_f32_i32 v168, v64\n"
      "v_cvt_f32_i32 v169, v68\n"
      "v_cvt_f32_i32 v170, v65\n"
      "v_cvt_f32_i32 v171, v69\n"
      "v_cvt_f32_i32 v172, v66\n"
      "v_cvt_f32_i32 v173, v70\n"
      "v_cvt_f32_i32 v174, v67\n"
      "v_cvt_f32_i32 v175, v71\n"
      "v_pk_fma_f32 v[168:169], v[168:169], v[164:165], v[160:161] op_sel:[0,0,0] op_sel_hi:[1,0,0] neg_lo:[0,0,1] neg_hi:[0,0,1]\n"
      "v_pk_fma_f32 v[170:171], v[170:171], v[164:165], v[160:161] op_sel:[0,1,1] op_sel_hi:[1,1,1] neg_lo:[0,0,1] neg_hi:[0,0,1]\n"
      "v_pk_fma_f32 v[172:173], v[172:173], v[166:167], v[162:163] op_sel:[0,0,0] op_sel_hi:[1,0,0] neg_lo:[0,0,1] neg_hi:[0,0,1]\n"
      "v_pk_fma_f32 v[174:175], v[174:175], v[166:167], v[162:163] op_sel:[0,1,1] op_sel_hi:[1,1,1] neg_lo:[0,0,1] neg_hi:[0,0,1]\n"
      "v_cvt_pk_bf16_f32 v130, v168, v169\n"
      "v_cvt_pk_bf16_f32 v138, v170, v171\n"
      "v_cvt_pk_bf16_f32 v134, v172, v173\n"
      "v_cvt_pk_bf16_f32 v142, v174, v175\n"
      "v_mov_b32 v4, v130\n"
      "v_mov_b32 v5, v134\n"
      "v_mov_b32_dpp v130, v138 row_ror:8 row_mask:0xf bank_mask:0xc\n"
      "v_mov_b32_dpp v134, v142 row_ror:8 row_mask:0xf bank_mask:0xc\n"
      "v_mov_b32_dpp v138, v4 row_ror:8 row_mask:0xf bank_mask:0x3\n"
      "v_mov_b32_dpp v142, v5 row_ror:8 row_mask:0xf bank_mask:0x3\n"
      "s_waitcnt vmcnt(10)\n"
      "buffer_load_dwordx4 v[56:59], v178, s[32:35], s53 offen sc1\n"
      "buffer_load_dwordx4 v[60:63], v179, s[32:35], s53 offen sc1\n"
      "v_cvt_f32_i32 v168, v72\n"
      "v_cvt_f32_i32 v169, v76\n"
      "v_cvt_f32_i32 v170, v73\n"
      "v_cvt_f32_i32 v171, v77\n"
      "v_cvt_f32_i32 v172, v74\n"
      "v_cvt_f32_i32 v173, v78\n"
      "v_cvt_f32_i32 v174, v75\n"
      "v_cvt_f32_i32 v175, v79\n"
      "v_pk_fma_f32 v[168:169], v[168:169], v[164:165], v[160:161] op_sel:[0,0,0] op_sel_hi:[1,0,0] neg_lo:[0,0,1] neg_hi:[0,0,1]\n"
      "v_pk_fma_f32 v[170:171], v[170:171], v[164:165], v[160:161] op_sel:[0,1,1] op_sel_hi:[1,1,1] neg_lo:[0,0,1] neg_hi:[0,0,1]\n"
      "v_pk_fma_f32 v[172:173], v[172:173], v[166:167], v[162:163] op_sel:[0,0,0] op_sel_hi:[1,0,0] neg_lo:[0,0,1] neg_hi:[0,0,1]\n"
      "v_pk_fma_f32 v[174:175], v[174:175], v[166:167], v[162:163] op_sel:[0,1,1] op_sel_hi:[1,1,1] neg_lo:[0,0,1] neg_hi:[0,0,1]\n"
      "v_cvt_pk_bf16_f32 v131, v168, v169\n"
      "v_cvt_pk_bf16_f32 v139, v170, v171\n"
      "v_cvt_pk_bf16_f32 v135, v172, v173\n"
      "v_cvt_pk_bf16_f32 v143, v174, v175\n"
      "v_mov_b32 v4, v131\n"
      "v_mov_b32 v5, v135\n"
      "v_mov_b32_dpp v131, v139 row_ror:8 row_mask:0xf bank_mask:0xc\n"
      "v_mov_b32_dpp v135, v143 row_ror:8 row_mask:0xf bank_mask:0xc\n"
      "v_mov_b32_dpp v139, v4 row_ror:8 row_mask:0xf bank_mask:0x3\n"
      "v_mov_b32_dpp v143, v5 row_ror:8 row_mask:0xf bank_mask:0x3\n"
      "s_mov_b32 s54, 262144\n"
      "s_add_u32 s62, s63, 0\n"
      "s_getpc_b64 s[56:57]\n"
      "s_waitcnt vmcnt(10)\n"
      "v_max3_u32 v4, v16, v17, v18\n"
      "v_max3_u32 v5, v19, v20, v21\n"
      "v_max3_u32 v4, v22, v23, v4\n"
      "v_max_u32 v4, v4, v5\n"
      "v_cmp_eq_u32 vcc, -1, v4\n"
      "s_cbranch_vccnz .Lq_xf0\n"
      "v_mfma_f32_16x16x32_bf16 v[180:183], v[80:83], v[16:19], 0\n"
      "s_add_u32 s53, s63, 12288\n"
      "buffer_load_dwordx4 v[64:67], v176, s[32:35], s53 offen sc1\n"
      "v_mfma_f32_16x16x32_bf16 v[184:187], v[84:87], v[16:19], 0\n"
      "v_mfma_f32_16x16x32_bf16 v[188:191], v[80:83], v[20:23], 0\n"
      "buffer_load_dwordx4 v[68:71], v177, s[32:35], s53 offen sc1\n"
      "v_mfma_f32_16x16x32_bf16 v[192:195], v[84:87], v[20:23], 0\n"
      "s_getpc_b64 s[56:57]\n"
      "s_waitcnt vmcnt(10)\n"
      "v_max3_u32 v4, v24, v25, v26\n"
      "v_max3_u32 v5, v27, v28, v29\n"
      "v_max3_u32 v4, v30, v31, v4\n"
      "v_max_u32 v4, v4, v5\n"
      "v_cmp_eq_u32 vcc, -1, v4\n"
      "s_cbranch_vccnz .Lq_xf1\n"
      "v_mfma_f32_16x16x32_bf16 v[196:199], v[80:83], v[24:27], 0\n"
      "buffer_load_dwordx4 v[72:75], v178, s[32:35], s53 offen sc1\n"
      "v_mfma_f32_16x16x32_bf16 v[200:203], v[84:87], v[24:27], 0\n"
      "v_mfma_f32_16x16x32_bf16 v[204:207], v[80:83], v[28:31], 0\n"
      "buffer_load_dwordx4 v[76:79], v179, s[32:35], s53 offen sc1\n"
      "v_mfma_f32_16x16x32_bf16 v[208:211], v[84:87], v[28:31], 0\n"
      "s_add_u32 s62, s63, 4096\n"
      "s_getpc_b64 s[56:57]\n"
      "s_waitcnt vmcnt(10)\n"
      "v_max3_u32 v4, v32, v33, v34\n"
      "v_max3_u32 v5, v35, v36, v37\n"
      "v_max3_u32 v4, v38, v39, v4\n"
      "v_max_u32 v4, v4, v5\n"
      "v_cmp_eq_u32 vcc, -1, v4\n"
      "s_cbranch_vccnz .Lq_xf2\n"
      "v_mfma_f32_16x16x32_bf16 v[180:183], v[88:91], v[32:35], v[180:183]\n"
      "s_add_u32 s53, s63, 16384\n"
      "buffer_load_dwordx4 v[16:19], v176, s[32:35], s53 offen sc1\n"
      "v_mfma_f32_16x16x32_bf16 v[184:187], v[92:95], v[32:35], v[184:187]\n"
      "v_mfma_f32_16x16x32_bf16 v[188:191], v[88:91], v[36:39], v[188:191]\n"
      "buffer_load_dwordx4 v[20:23], v177, s[32:35], s53 offen sc1\n"
      "v_mfma_f32_16x16x32_bf16 v[192:195], v[92:95], v[36:39], v[192:195]\n"
      "s_getpc_b64 s[56:57]\n"
      "s_waitcnt vmcnt(10)\n"
      "v_max3_u32 v4, v40, v41, v42\n"
      "v_max3_u32 v5, v43, v44, v45\n"
      "v_max3_u32 v4, v46, v47, v4\n"
      "v_max_u32 v4, v4, v5\n"
      "v_cmp_eq_u32 vcc, -1, v4\n"
      "s_cbranch_vccnz .Lq_xf3\n"
      "v_mfma_f32_16x16x32_bf16 v[196:199], v[88:91], v[40:43], v[196:199]\n"
      "buffer_load_dwordx4 v[24:27], v178, s[32:35], s53 offen sc1\n"
      "v_mfma_f32_16x16x32_bf16 v[200:203], v[92:95], v[40:43], v[200:203]\n"
      "v_mfma_f32_16x16x32_bf16 v[204:207], v[88:91], v[44:47], v[204:207]\n"
      "buffer_load_dwordx4 v[28:31], v179, s[32:35], s53 offen sc1\n"
      "v_mfma_f32_16x16x32_bf16 v[208:211], v[92:95], v[44:47], v[208:211]\n"
      "s_add_u32 s62, s63, 8192\n"
      "s_getpc_b64 s[56:57]\n"
      "s_waitcnt vmcnt(10)\n"
      "v_max3_u32 v4, v48, v49, v50\n"
      "v_max3_u32 v5, v51, v52, v53\n"
      "v_max3_u32 v4, v54, v55, v4\n"
      "v_max_u32 v4, v4, v5\n"
      "v_cmp_eq_u32 vcc, -1, v4\n"
      "s_cbranch_vccnz .Lq_xf4\n"
      "v_mfma_f32_16x16x32_bf16 v[180:183], v[96:99], v[48:51], v[180:183]\n"
      "s_add_u32 s53, s63, 20480\n"
      "buffer_load_dwordx4 v[32:35], v176, s[32:35], s53 offen sc1\n"
      "v_mfma_f32_16x16x32_bf16 v[184:187], v[100:103], v[48:51], v[184:187]\n"
      "v_mfma_f32_16x16x32_bf16 v[188:191], v[96:99], v[52:55], v[188:191]\n"
      "buffer_load_dwordx4 v[36:39], v177, s[32:35], s53 offen sc1\n"
      "v_mfma_f32_16x16x32_bf16 v[192:195], v[100:103], v[52:55], v[192:195]\n"
      "s_getpc_b64 s[56:57]\n"
      "s_waitcnt vmcnt(10)\n"
      "v_max3_u32 v4, v56, v57, v58\n"
      "v_max3_u32 v5, v59, v60, v61\n"
      "v_max3_u32 v4, v62, v63, v4\n"
      "v_max_u32 v4, v4, v5\n"
      "v_cmp_eq_u32 vcc, -1, v4\n"
      "s_cbranch_vccnz .Lq_xf5\n"
      "v_mfma_f32_16x16x32_bf16 v[196:199], v[96:99], v[56:59], v[196:199]\n"
      "buffer_load_dwordx4 v[40:43], v178, s[32:35], s53 offen sc1\n"
      "v_mfma_f32_16x16x32_bf16 v[200:203], v[100:103], v[56:59], v[200:203]\n"
      "v_mfma_f32_16x16x32_bf16 v[204:207], v[96:99], v[60:63], v[204:207]\n"
      "buffer_load_dwordx4 v[44:47], v179, s[32:35], s53 offen sc1\n"
      "v_mfma_f32_16x16x32_bf16 v[208:211], v[100:103], v[60:63], v[208:211]\n"
      "s_add_u32 s62, s63, 12288\n"
      "s_getpc_b64 s[56:57]\n"
      "s_waitcnt vmcnt(10)\n"
      "v_max3_u32 v4, v64, v65, v66\n"
      "v_max3_u32 v5, v67, v68, v69\n"
      "v_max3_u32 v4, v70, v71, v4\n"
      "v_max_u32 v4, v4, v5\n"
      "v_cmp_eq_u32 vcc, -1, v4\n"
      "s_cbranch_vccnz .Lq_xf6\n"
      "v_mfma_f32_16x16x32_bf16 v[180:183], v[104:107], v[64:67], v[180:183]\n"
      "s_add_u32 s53, s63, 24576\n"
      "buffer_load_dwordx4 v[48:51], v176, s[32:35], s53 offen sc1\n"
      "v_mfma_f32_16x16x32_bf16 v[184:187], v[108:111], v[64:67], v[184:187]\n"
      "v_mfma_f32_16x16x32_bf16 v[188:191], v[104:107], v[68:71], v[188:191]\n"
      "buffer_load_dwordx4 v[52:55], v177, s[32:35], s53 offen sc1\n"
      "v_mfma_f32_16x16x32_bf16 v[192:195], v[108:111], v[68:71], v[192:195]\n"
      "s_getpc_b64 s[56:57]\n"
      "s_waitcnt vmcnt(10)\n"
      "v_max3_u32 v4, v72, v73, v74\n"
      "v_max3_u32 v5, v75, v76, v77\n"
      "v_max3_u32 v4, v78, v79, v4\n"
      "v_max_u32 v4, v4, v5\n"
      "v_cmp_eq_u32 vcc, -1, v4\n"
      "s_cbranch_vccnz .Lq_xf7\n"
      "v_mfma_f32_16x16x32_bf16 v[196:199], v[104:107], v[72:75], v[196:199]\n"
      "buffer_load_dwordx4 v[56:59], v178, s[32:35], s53 offen sc1\n"
      "v_mfma_f32_16x16x32_bf16 v[200:203], v[108:111], v[72:75], v[200:203]\n"
      "v_mfma_f32_16x16x32_bf16 v[204:207], v[104:107], v[76:79], v[204:207]\n"
      "buffer_load_dwordx4 v[60:63], v179, s[32:35], s53 offen sc1\n"
      "v_mfma_f32_16x16x32_bf16 v[208:211], v[108:111], v[76:79], v[208:211]\n"
      "s_add_u32 s62, s63, 16384\n"
      "s_getpc_b64 s[56:57]\n"
      "s_waitcnt vmcnt(10)\n"
      "v_max3_u32 v4, v16, v17, v18\n"
      "v_max3_u32 v5, v19, v20, v21\n"
      "v_max3_u32 v4, v22, v23, v4\n"
      "v_max_u32 v4, v4, v5\n"
      "v_cmp_eq_u32 vcc, -1, v4\n"
      "s_cbranch_vccnz .Lq_xf0\n"
      "v_mfma_f32_16x16x32_bf16 v[180:183], v[112:115], v[16:19], v[180:183]\n"
      "s_add_u32 s53, s63, 28672\n"
      "buffer_load_dwordx4 v[64:67], v176, s[32:35], s53 offen sc1\n"
      "v_mfma_f32_16x16x32_bf16 v[184:187], v[116:119], v[16:19], v[184:187]\n"
      "v_mfma_f32_16x16x32_bf16 v[188:191], v[112:115], v[20:23], v[188:191]\n"
      "buffer_load_dwordx4 v[68:71], v177, s[32:35], s53 offen sc1\n"
      "v_mfma_f32_16x16x32_bf16 v[192:195], v[116:119], v[20:23], v[192:195]\n"
      "s_getpc_b64 s[56:57]\n"
      "s_waitcnt vmcnt(10)\n"
      "v_max3_u32 v4, v24, v25, v26\n"
      "v_max3_u32 v5, v27, v28, v29\n"
      "v_max3_u32 v4, v30, v31, v4\n"
      "v_max_u32 v4, v4, v5\n"
      "v_cmp_eq_u32 vcc, -1, v4\n"
      "s_cbranch_vccnz .Lq_xf1\n"
      "v_mfma_f32_16x16x32_bf16 v[196:199], v[112:115], v[24:27], v[196:199]\n"
      "buffer_load_dwordx4 v[72:75], v178, s[32:35], s53 offen sc1\n"
      "v_mfma_f32_16x16x32_bf16 v[200:203], v[116:119], v[24:27], v[200:203]\n"
      "v_mfma_f32_16x16x32_bf16 v[204:207], v[112:115], v[28:31], v[204:207]\n"
      "buffer_load_dwordx4 v[76:79], v179, s[32:35], s53 offen sc1\n"
      "v_mfma_f32_16x16x32_bf16 v[208:211], v[116:119], v[28:31], v[208:211]\n"
      "s_add_u32 s62, s63, 20480\n"
      "s_getpc_b64 s[56:57]\n"
      "s_waitcnt vmcnt(10)\n"
      "v_max3_u32 v4, v32, v33, v34\n"
      "v_max3_u32 v5, v35, v36, v37\n"
      "v_max3_u32 v4, v38, v39, v4\n"
      "v_max_u32 v4, v4, v5\n"
      "v_cmp_eq_u32 vcc, -1, v4\n"
      "s_cbranch_vccnz .Lq_xf2\n"
      "v_mfma_f32_16x16x32_bf16 v[180:183], v[120:123], v[32:35], v[180:183]\n"
      "v_mfma_f32_16x16x32_bf16 v[184:187], v[124:127], v[32:35], v[184:187]\n"
      "v_mfma_f32_16x16x32_bf16 v[188:191], v[120:123], v[36:39], v[188:191]\n"
      "v_mfma_f32_16x16x32_bf16 v[192:195], v[124:127], v[36:39], v[192:195]\n"
      "s_getpc_b64 s[56:57]\n"
      "s_waitcnt vmcnt(8)\n"
      "v_max3_u32 v4, v40, v41, v42\n"
      "v_max3_u32 v5, v43, v44, v45\n"
      "v_max3_u32 v4, v46, v47, v4\n"
      "v_max_u32 v4, v4, v5\n"
      "v_cmp_eq_u32 vcc, -1, v4\n"
      "s_cbranch_vccnz .Lq_xf3\n"
      "v_mfma_f32_16x16x32_bf16 v[196:199], v[120:123], v[40:43], v[196:199]\n"
      "v_mfma_f32_16x16x32_bf16 v[200:203], v[124:127], v[40:43], v[200:203]\n"
      "v_mfma_f32_16x16x32_bf16 v[204:207], v[120:123], v[44:47], v[204:207]\n"
      "v_mfma_f32_16x16x32_bf16 v[208:211], v[124:127], v[44:47], v[208:211]\n"
      "v_mov_b32 v2, 28\n"
      "ds_write_b32 v7, v2\n"
      "s_add_u32 s62, s63, 24576\n"
      "s_getpc_b64 s[56:57]\n"
      "s_waitcnt vmcnt(6)\n"
      "v_max3_u32 v4, v48, v49, v50\n"
      "v_max3_u32 v5, v51, v52, v53\n"
      "v_max3_u32 v4, v54, v55, v4\n"
      "v_max_u32 v4, v4, v5\n"
      "v_cmp_eq_u32 vcc, -1, v4\n"
      "s_cbranch_vccnz .Lq_xf4\n"
      "v_mfma_f32_16x16x32_bf16 v[180:183], v[128:131], v[48:51], v[180:183]\n"
      "v_mfma_f32_16x16x32_bf16 v[184:187], v[132:135], v[48:51], v[184:187]\n"
      "v_mfma_f32_16x16x32_bf16 v[188:191], v[128:131], v[52:55], v[188:191]\n"
      "v_mfma_f32_16x16x32_bf16 v[192:195], v[132:135], v[52:55], v[192:195]\n"
      "s_getpc_b64 s[56:57]\n"
      "s_waitcnt vmcnt(4)\n"
      "v_max3_u32 v4, v56, v57, v58\n"
      "v_max3_u32 v5, v59, v60, v61\n"
      "v_max3_u32 v4, v62, v63, v4\n"
      "v_max_u32 v4, v4, v5\n"
      "v_cmp_eq_u32 vcc, -1, v4\n"
      "s_cbranch_vccnz .Lq_xf5\n"
      "v_mfma_f32_16x16x32_bf16 v[196:199], v[128:131], v[56:59], v[196:199]\n"
      "v_mfma_f32_16x16x32_bf16 v[200:203], v[132:135], v[56:59], v[200:203]\n"
      "v_mfma_f32_16x16x32_bf16 v[204:207], v[128:131], v[60:63], v[204:207]\n"
      "v_mfma_f32_16x16x32_bf16 v[208:211], v[132:135], v[60:63], v[208:211]\n"
      "s_add_u32 s50, s36, 1\n"
      "s_and_b32 s50, s50, 3\n"
      "s_mul_i32 s50, s50, 6144\n"
      "v_add_u32 v9, s50, v3\n"
      "s_add_u32 s50, s36, 2\n"
      "s_and_b32 s50, s50, 3\n"
      "s_mul_i32 s50, s50, 6144\n"
      "v_add_u32 v10, s50, v3\n"
      "s_add_u32 s50, s36, 3\n"
      "s_and_b32 s50, s50, 3\n"
      "s_mul_i32 s50, s50, 6144\n"
      "v_add_u32 v11, s50, v3\n"
      "s_mul_i32 s50, s36, 6144\n"
      "v_add_u32 v8, s50, v3\n"
      "v_and_b32 v1, 3, v0\n"
      "v_lshl_add_u32 v1, v1, 8, v6\n"
      "s_add_u32 s62, s63, 28672\n"
      "s_getpc_b64 s[56:57]\n"
      "s_waitcnt vmcnt(2)\n"
      "v_max3_u32 v4, v64, v65, v66\n"
      "v_max3_u32 v5, v67, v68, v69\n"
      "v_max3_u32 v4, v70, v71, v4\n"
      "v_max_u32 v4, v4, v5\n"
      "v_cmp_eq_u32 vcc, -1, v4\n"
      "s_cbranch_vccnz .Lq_xf6\n"
      "v_mfma_f32_16x16x32_bf16 v[180:183], v[136:139], v[64:67], v[180:183]\n"
      "v_mfma_f32_16x16x32_bf16 v[184:187], v[140:143], v[64:67], v[184:187]\n"
      "v_mfma_f32_16x16x32_bf16 v[188:191], v[136:139], v[68:71], v[188:191]\n"
      "v_mfma_f32_16x16x32_bf16 v[192:195], v[140:143], v[68:71], v[192:195]\n"
      "s_getpc_b64 s[56:57]\n"
      "s_waitcnt vmcnt(0)\n"
      "v_max3_u32 v4, v72, v73, v74\n"
      "v_max3_u32 v5, v75, v76, v77\n"
      "v_max3_u32 v4, v78, v79, v4\n"
      "v_max_u32 v4, v4, v5\n"
      "v_cmp_eq_u32 vcc, -1, v4\n"
      "s_cbranch_vccnz .Lq_xf7\n"
      "v_mfma_f32_16x16x32_bf16 v[196:199], v[136:139], v[72:75], v[196:199]\n"
      "v_mfma_f32_16x16x32_bf16 v[200:203], v[140:143], v[72:75], v[200:203]\n"
      "v_mfma_f32_16x16x32_bf16 v[204:207], v[136:139], v[76:79], v[204:207]\n"
      "v_mfma_f32_16x16x32_bf16 v[208:211], v[140:143], v[76:79], v[208:211]\n"
      "s_nop 2\n"
      "s_mul_i32 s52, s36, 112\n"
      "s_add_u32 s52, s52, s39\n"
      "ds_write_b128 v9, v[188:191] offset:0\n"
      "ds_write_b128 v9, v[192:195] offset:1024\n"
      "ds_write_b128 v10, v[196:199] offset:2048\n"
      "ds_write_b128 v10, v[200:203] offset:3072\n"
      "ds_write_b128 v11, v[204:207] offset:4096\n"
      "ds_write_b128 v11, v[208:211] offset:5120\n"
      "v_mov_b32 v2, 64\n"
      "s_lshl_b32 s52, s52, 10\n"
      "s_add_u32 s52, s52, 196608\n"
      "s_mov_b32 s54, 262144\n"
      "v_mov_b32 v5, 0x7fff7fff\n"
      "ds_write_b32 v7, v2\n"
      ".Lq_rs:\n"
      "ds_read_b32 v2, v1\n"
      "s_sub_u32 s54, s54, 1\n"
      "s_cbranch_scc1 .Lq_rse\n"
      "s_waitcnt lgkmcnt(0)\n"
      "v_cmp_gt_u32 vcc, 64, v2\n"
      "s_cbranch_vccnz .Lq_rs\n"
      ".Lq_rse:\n"
      "ds_read_b128 v[16:19], v8 offset:0\n"
      "ds_read_b128 v[20:23], v8 offset:1024\n"
      "ds_read_b128 v[24:27], v8 offset:2048\n"
      "ds_read_b128 v[28:31], v8 offset:3072\n"
      "ds_read_b128 v[32:35], v8 offset:4096\n"
      "ds_read_b128 v[36:39], v8 offset:5120\n"
      "s_waitcnt lgkmcnt(4)\n"
      "v_pk_add_f32 v[180:181], v[180:181], v[16:17]\n"
      "v_pk_add_f32 v[182:183], v[182:183], v[18:19]\n"
      "v_pk_add_f32 v[184:185], v[184:185], v[20:21]\n"
      "v_pk_add_f32 v[186:187], v[186:187], v[22:23]\n"
      "s_waitcnt lgkmcnt(2)\n"
      "v_pk_add_f32 v[180:181], v[180:181], v[24:25]\n"
      "v_pk_add_f32 v[182:183], v[182:183], v[26:27]\n"
      "v_pk_add_f32 v[184:185], v[184:185], v[28:29]\n"
      "v_pk_add_f32 v[186:187], v[186:187], v[30:31]\n"
      "s_waitcnt lgkmcnt(0)\n"
      "v_pk_add_f32 v[180:181], v[180:181], v[32:33]\n"
      "v_pk_add_f32 v[182:183], v[182:183], v[34:35]\n"
      "v_pk_add_f32 v[184:185], v[184:185], v[36:37]\n"
      "v_pk_add_f32 v[186:187], v[186:187], v[38:39]\n"
      "v_cvt_pk_bf16_f32 v12, v180, v181\n"
      "v_cvt_pk_bf16_f32 v13, v182, v183\n"
      "v_cvt_pk_bf16_f32 v14, v184, v185\n"
      "v_cvt_pk_bf16_f32 v15, v186, v187\n"
      "v_cmp_ne_u32 s[46:47], -1, v12\n"
      "v_cmp_ne_u32 s[48:49], -1, v13\n"
      "s_nop 0\n"
      "v_cndmask_b32 v12, v5, v12, s[46:47]\n"
      "v_cndmask_b32 v13, v5, v13, s[48:49]\n"
      "v_cmp_ne_u32 s[46:47], -1, v14\n"
      "v_cmp_ne_u32 s[48:49], -1, v15\n"
      "s_nop 0\n"
      "v_cndmask_b32 v14, v5, v14, s[46:47]\n"
      "v_cndmask_b32 v15, v5, v15, s[48:49]\n"
      "buffer_store_dwordx4 v[12:15], v3, s[32:35], s52 offen sc1\n"
      "v_mov_b32 v2, 65\n"
      "ds_write_b32 v7, v2\n"
      "s_lshr_b32 s41, s37, 2\n"
      "s_mul_i32 s43, s41, 56\n"
      "s_add_u32 s43, s43, 28\n"
      "s_lshl_b32 s41, s41, 1\n"
      "s_movk_i32 s53, 11\n"
      ".Lq_s3:\n"
      "ds_read_b32 v5, v6 offset:1536\n"
      "s_mov_b32 s56, 0x65432180\n"
      "s_mov_b32 s57, 0xdcba97\n"
      "s_lshl_b32 s50, s40, 2\n"
      "s_lshr_b64 s[56:57], s[56:57], s50\n"
      "s_and_b32 s42, s56, 15\n"
      "s_and_b32 s51, s37, 3\n"
      "s_mul_i32 s51, s51, 14\n"
      "s_lshr_b32 s50, s24, 7\n"
      "s_add_u32 s50, s50, s51\n"
      "s_sub_u32 s50, 3, s50\n"
      "s_and_b32 s50, s50, 7\n"
      "s_add_u32 s42, s42, s50\n"
      "s_cmp_ge_u32 s42, 14\n"
      "s_cselect_b32 s50, 14, 0\n"
      "s_sub_u32 s42, s42, s50\n"
      "s_add_u32 s42, s42, s51\n"
      "s_lshr_b32 s52, s36, 1\n"
      "s_and_b32 s46, s36, 1\n"
      "s_lshl_b32 s46, s46, 1\n"
      "s_add_u32 s46, s46, s52\n"
      "s_mul_i32 s53, s53, s52\n"
      "s_add_u32 s53, s53, s43\n"
      "s_mul_i32 s50, s46, 112\n"
      "s_add_u32 s50, s50, s53\n"
      "s_lshl_b32 s50, s50, 10\n"
      "s_add_u32 s58, s50, 196608\n"
      "s_xor_b32 s50, s46, 1\n"
      "s_mul_i32 s50, s50, 112\n"
      "s_add_u32 s50, s50, s53\n"
      "s_lshl_b32 s50, s50, 10\n"
      "s_add_u32 s59, s50, 196608\n"
      "v_lshrrev_b32 v1, 5, v0\n"
      "v_bfe_u32 v2, v0, 2, 3\n"
      "v_and_b32 v4, 3, v0\n"
      "v_lshlrev_b32 v4, 3, v4\n"
      "v_lshl_add_u32 v4, v1, 8, v4\n"
      "v_and_b32 v1, 1, v2\n"
      "v_lshl_add_u32 v4, v1, 7, v4\n"
      "v_lshrrev_b32 v1, 1, v2\n"
      "v_lshl_add_u32 v4, v1, 10, v4\n"
      "s_lshl_b32 s51, s52, 12\n"
      "v_add_u32 v4, s51, v4\n"
      "v_lshrrev_b32 v2, 1, v2\n"
      "s_add_u32 s50, s41, 0\n"
      "s_and_b32 s50, s50, 3\n"
      "v_xor_b32 v1, s50, v2\n"
      "v_lshl_add_u32 v12, v1, 5, v4\n"
      "s_add_u32 s50, s41, 1\n"
      "s_and_b32 s50, s50, 3\n"
      "v_xor_b32 v1, s50, v2\n"
      "v_lshl_add_u32 v13, v1, 5, v4\n"
      "s_add_u32 s50, s41, 2\n"
      "s_and_b32 s50, s50, 3\n"
      "v_xor_b32 v1, s50, v2\n"
      "v_lshl_add_u32 v14, v1, 5, v4\n"
      "s_add_u32 s50, s41, 3\n"
      "s_and_b32 s50, s50, 3\n"
      "v_xor_b32 v1, s50, v2\n"
      "v_lshl_add_u32 v15, v1, 5, v4\n"
      "s_lshl_b32 s53, s42, 4\n"
      "s_add_u32 s53, s53, s46\n"
      "s_add_u32 s50, s41, 0\n"
      "s_and_b32 s50, s50, 3\n"
      "s_lshl_b32 s50, s50, 2\n"
      "s_add_u32 s50, s50, s53\n"
      "s_mul_i32 s50, s50, 3072\n"
      "s_add_u32 s4, s50, 655360\n"
      "s_add_u32 s50, s41, 1\n"
      "s_and_b32 s50, s50, 3\n"
      "s_lshl_b32 s50, s50, 2\n"
      "s_add_u32 s50, s50, s53\n"
      "s_mul_i32 s50, s50, 3072\n"
      "s_add_u32 s5, s50, 655360\n"
      "s_add_u32 s50, s41, 2\n"
      "s_and_b32 s50, s50, 3\n"
      "s_lshl_b32 s50, s50, 2\n"
      "s_add_u32 s50, s50, s53\n"
      "s_mul_i32 s50, s50, 3072\n"
      "s_add_u32 s6, s50, 655360\n"
      "s_add_u32 s50, s41, 3\n"
      "s_and_b32 s50, s50, 3\n"
      "s_lshl_b32 s50, s50, 2\n"
      "s_add_u32 s50, s50, s53\n"
      "s_mul_i32 s50, s50, 3072\n"
      "s_add_u32 s7, s50, 655360\n"
      "v_and_b32 v1, 15, v0\n"
      "v_lshrrev_b32 v2, 4, v0\n"
      "v_mul_u32_u24 v1, 7168, v1\n"
      "v_lshl_add_u32 v216, v2, 3, v1\n"
      "s_mul_i32 s50, s46, 114688\n"
      "s_lshl_b32 s51, s42, 7\n"
      "s_add_u32 s50, s50, s51\n"
      "s_lshl_b32 s51, s41, 5\n"
      "s_add_u32 s8, s50, s51\n"
      "s_lshl_b32 s50, s36, 12\n"
      "s_add_u32 s51, s50, 139264\n"
      "v_add_u32 v217, s51, v3\n"
      "s_xor_b32 s50, s50, 0x2000\n"
      "s_add_u32 s51, s50, 139264\n"
      "v_add_u32 v218, s51, v3\n"
      "v_xor_b32 v219, 0x200, v7\n"
      "v_mov_b32 v220, 0xffffff00\n"
      "ds_read_b32 v222, v6 offset:1536\n"
      "ds_read_b32 v223, v6 offset:1792\n"
      "v_add_u32 v1, s41, v0\n"
      "v_add_u32 v1, 1, v1\n"
      "v_and_b32 v1, 3, v1\n"
      "v_mul_u32_u24 v1, 384, v1\n"
      "v_lshl_add_u32 v1, v0, 7, v1\n"
      "s_mul_i32 s50, s42, 1536\n"
      "s_add_u32 s50, s50, 3407872\n"
      "v_add_u32 v1, s50, v1\n"
      "s_cmp_eq_u32 s36, 0\n"
      "s_cselect_b32 s50, 3, 0\n"
      "v_cmp_gt_u32 vcc, s50, v0\n"
      "v_mov_b32 v2, 0x7ffffff0\n"
      "s_nop 0\n"
      "v_cndmask_b32 v221, v2, v1, vcc\n"
      "s_waitcnt lgkmcnt(0)\n"
      "v_readfirstlane_b32 s44, v5\n"
      "s_bfe_u32 s12, s44, 0x80000\n"
      "s_bfe_u32 s13, s44, 0x80008\n"
      "s_bfe_u32 s14, s44, 0x80010\n"
      "s_bfe_u32 s15, s44, 0x80018\n"
      "s_bitcmp1_b32 s41, 0\n"
      "s_cbranch_scc1 .Lq_s3b\n"
      "s_mov_b32 s54, 262144\n"
      ".Lq_spin5:\n"
      "ds_read_b32 v2, v6 offset:1024\n"
      "s_sub_u32 s54, s54, 1\n"
      "s_cbranch_scc1 .Lq_spe6\n"
      "s_waitcnt lgkmcnt(0)\n"
      "v_readfirstlane_b32 s55, v2\n"
      "s_cmp_ge_u32 s55, 26\n"
      "s_cbranch_scc1 .Lq_spe6\n"
      "s_sleep 1\n"
      "s_branch .Lq_spin5\n"
      ".Lq_spe6:\n"
      "s_mov_b32 s54, 262144\n"
      ".Lq_s3ra:\n"
      "s_add_u32 s48, s58, 4096\n"
      "buffer_load_dwordx4 v[64:67], v3, s[32:35], s48 offen offset:2048 sc1\n"
      "s_add_u32 s49, s59, 4096\n"
      "buffer_load_dwordx4 v[68:71], v3, s[32:35], s49 offen offset:2048 sc1\n"
      "buffer_load_dwordx4 v[72:75], v3, s[32:35], s48 offen offset:3072 sc1\n"
      "buffer_load_dwordx4 v[76:79], v3, s[32:35], s49 offen offset:3072 sc1\n"
      "s_add_u32 s48, s58, 8192\n"
      "buffer_load_dwordx4 v[80:83], v3, s[32:35], s48 offen offset:0 sc1\n"
      "s_add_u32 s49, s59, 8192\n"
      "buffer_load_dwordx4 v[84:87], v3, s[32:35], s49 offen offset:0 sc1\n"
      "buffer_load_dwordx4 v[88:91], v3, s[32:35], s48 offen offset:1024 sc1\n"
      "buffer_load_dwordx4 v[92:95], v3, s[32:35], s49 offen offset:1024 sc1\n"
      "buffer_load_dwordx4 v[96:99], v3, s[32:35], s48 offen offset:2048 sc1\n"
      "buffer_load_dwordx4 v[100:103], v3, s[32:35], s49 offen offset:2048 sc1\n"
      "buffer_load_dwordx4 v[104:107], v3, s[32:35], s48 offen offset:3072 sc1\n"
      "buffer_load_dwordx4 v[108:111], v3, s[32:35], s49 offen offset:3072 sc1\n"
      "v_add_u32 v9, 0, v13\n"
      "v_add_u32 v10, 0, v14\n"
      "v_add_u32 v11, 0, v15\n"
      "v_add_u32 v8, 131072, v12\n"
      "ds_read_b64_tr_b16 v[156:157], v9 offset:49152\n"
      "ds_read_b64_tr_b16 v[158:159], v9 offset:49664\n"
      "ds_read_b64_tr_b16 v[160:161], v10 offset:49152\n"
      "ds_read_b64_tr_b16 v[162:163], v10 offset:49664\n"
      "ds_read_b64_tr_b16 v[164:165], v11 offset:49152\n"
      "ds_read_b64_tr_b16 v[166:167], v11 offset:49664\n"
      "s_waitcnt vmcnt(10)\n"
      "v_cmp_u_f32 vcc, v64, v68\n"
      "s_cbranch_vccnz .Lq_hs7\n"
      ".Lq_hb8:\n"
      "s_waitcnt lgkmcnt(2)\n"
      "v_mfma_f32_16x16x32_bf16 v[204:207], v[156:159], v[68:71], 0\n"
      "ds_read_b64_tr_b16 v[172:173], v9 offset:57344\n"
      "s_add_u32 s48, s58, 12288\n"
      "buffer_load_dwordx4 v[112:115], v3, s[32:35], s48 offen offset:0 sc1\n"
      "v_mfma_f32_16x16x32_bf16 v[188:191], v[156:159], v[64:67], 0\n"
      "ds_read_b64_tr_b16 v[174:175], v9 offset:57856\n"
      "s_add_u32 s49, s59, 12288\n"
      "buffer_load_dwordx4 v[116:119], v3, s[32:35], s49 offen offset:0 sc1\n"
      "v_mfma_f32_16x16x32_bf16 v[208:211], v[160:163], v[68:71], 0\n"
      "ds_read_b64_tr_b16 v[176:177], v10 offset:57344\n"
      "v_mfma_f32_16x16x32_bf16 v[192:195], v[160:163], v[64:67], 0\n"
      "ds_read_b64_tr_b16 v[178:179], v10 offset:57856\n"
      "s_waitcnt vmcnt(10) lgkmcnt(4)\n"
      "v_mfma_f32_16x16x32_bf16 v[212:215], v[164:167], v[68:71], 0\n"
      "v_cmp_u_f32 vcc, v72, v76\n"
      "ds_read_b64_tr_b16 v[180:181], v11 offset:57344\n"
      "v_mfma_f32_16x16x32_bf16 v[196:199], v[164:167], v[64:67], 0\n"
      "ds_read_b64_tr_b16 v[182:183], v11 offset:57856\n"
      "s_cbranch_vccnz .Lq_hs9\n"
      ".Lq_hb10:\n"
      "s_branch .Lq_s3a\n"
      ".Lq_s3b:\n"
      "s_cmp_eq_u32 s36, 1\n"
      "s_cbranch_scc0 .Lq_bm1\n"
      "s_mov_b32 s54, 262144\n"
      ".Lq_spin11:\n"
      "ds_read_b32 v2, v6 offset:1024\n"
      "s_sub_u32 s54, s54, 1\n"
      "s_cbranch_scc1 .Lq_spe12\n"
      "s_waitcnt lgkmcnt(0)\n"
      "v_readfirstlane_b32 s55, v2\n"
      "s_cmp_ge_u32 s55, 2\n"
      "s_cbranch_scc1 .Lq_spe12\n"
      "s_sleep 4\n"
      "s_branch .Lq_spin11\n"
      ".Lq_spe12:\n"
      "v_readfirstlane_b32 s45, v223\n"
      "v_and_b32 v4, 0xf0, v3\n"
      "s_movk_i32 s50, 5888\n"
      "s_mov_b32 s54, 262144\n"
      ".Lq_fp13:\n"
      "buffer_load_dwordx2 v[16:17], v4, s[32:35], s50 offen sc1\n"
      "s_sub_u32 s54, s54, 1\n"
      "s_cbranch_scc1 .Lq_fpe14\n"
      "s_waitcnt vmcnt(0)\n"
      "v_cmp_ne_u64 vcc, s[44:45], v[16:17]\n"
      "s_cbranch_vccz .Lq_fpe14\n"
      "s_sleep 8\n"
      "s_branch .Lq_fp13\n"
      ".Lq_fpe14:\n"
      "s_cmp_lt_u32 s38, 16\n"
      "s_cselect_b32 s52, 8, 20\n"
      "s_mov_b32 s54, 262144\n"
      ".Lq_spin15:\n"
      "ds_read_b32 v2, v6 offset:1024\n"
      "s_sub_u32 s54, s54, 1\n"
      "s_cbranch_scc1 .Lq_spe16\n"
      "s_waitcnt lgkmcnt(0)\n"
      "v_readfirstlane_b32 s55, v2\n"
      "s_cmp_ge_u32 s55, s52\n"
      "s_cbranch_scc1 .Lq_spe16\n"
      "s_sleep 2\n"
      "s_branch .Lq_spin15\n"
      ".Lq_spe16:\n"
      "s_mul_i32 s50, s37, 14\n"
      "s_add_u32 s50, s50, s40\n"
      "s_lshl_b32 s50, s50, 8\n"
      "s_add_u32 s50, s50, 16384\n"
      "s_add_u32 s16, s32, s50\n"
      "s_addc_u32 s17, s33, 0\n"
      "s_mov_b32 s18, s44\n"
      "s_mov_b32 s19, s44\n"
      "s_movk_i32 s54, 64\n"
      ".Lq_mb:\n"
      "s_load_dwordx16 s[64:79], s[16:17], 0x0 glc\n"
      "s_load_dwordx16 s[80:95], s[16:17], 0x40 glc\n"
      "s_load_dwordx2 s[96:97], s[16:17], 0x80 glc\n"
      "s_sub_u32 s54, s54, 1\n"
      "s_cbranch_scc1 .Lq_mbe\n"
      "s_mov_b64 vcc, 0\n"
      "s_waitcnt lgkmcnt(0)\n"
      "s_xor_b64 s[64:65], s[64:65], s[18:19]\n"
      "s_or_b64 vcc, vcc, s[64:65]\n"
      "s_xor_b64 s[66:67], s[66:67], s[18:19]\n"
      "s_or_b64 vcc, vcc, s[66:67]\n"
      "s_xor_b64 s[68:69], s[68:69], s[18:19]\n"
      "s_or_b64 vcc, vcc, s[68:69]\n"
      "s_xor_b64 s[70:71], s[70:71], s[18:19]\n"
      "s_or_b64 vcc, vcc, s[70:71]\n"
      "s_xor_b64 s[72:73], s[72:73], s[18:19]\n"
      "s_or_b64 vcc, vcc, s[72:73]\n"
      "s_xor_b64 s[74:75], s[74:75], s[18:19]\n"
      "s_or_b64 vcc, vcc, s[74:75]\n"
      "s_xor_b64 s[76:77], s[76:77], s[18:19]\n"
      "s_or_b64 vcc, vcc, s[76:77]\n"
      "s_xor_b64 s[78:79], s[78:79], s[18:19]\n"
      "s_or_b64 vcc, vcc, s[78:79]\n"
      "s_xor_b64 s[80:81], s[80:81], s[18:19]\n"
      "s_or_b64 vcc, vcc, s[80:81]\n"
      "s_xor_b64 s[82:83], s[82:83], s[18:19]\n"
      "s_or_b64 vcc, vcc, s[82:83]\n"
      "s_xor_b64 s[84:85], s[84:85], s[18:19]\n"
      "s_or_b64 vcc, vcc, s[84:85]\n"
      "s_xor_b64 s[86:87], s[86:87], s[18:19]\n"
      "s_or_b64 vcc, vcc, s[86:87]\n"
      "s_xor_b64 s[88:89], s[88:89], s[18:19]\n"
      "s_or_b64 vcc, vcc, s[88:89]\n"
      "s_xor_b64 s[90:91], s[90:91], s[18:19]\n"
      "s_or_b64 vcc, vcc, s[90:91]\n"
      "s_xor_b64 s[92:93], s[92:93], s[18:19]\n"
      "s_or_b64 vcc, vcc, s[92:93]\n"
      "s_xor_b64 s[94:95], s[94:95], s[18:19]\n"
      "s_or_b64 vcc, vcc, s[94:95]\n"
      "s_xor_b64 s[96:97], s[96:97], s[18:19]\n"
      "s_or_b64 vcc, vcc, s[96:97]\n"
      "s_cbranch_scc0 .Lq_mbe\n"
      "s_sleep 4\n"
      "s_branch .Lq_mb\n"
      ".Lq_mbe:\n"
      "s_waitcnt lgkmcnt(0)\n"
      "v_mov_b32 v2, 2\n"
      "ds_write_b32 v6, v2 offset:1280\n"
      ".Lq_bm1:\n"
      "s_mov_b32 s54, 262144\n"
      ".Lq_spin17:\n"
      "ds_read_b32 v2, v6 offset:1280\n"
      "s_sub_u32 s54, s54, 1\n"
      "s_cbranch_scc1 .Lq_spe18\n"
      "s_waitcnt lgkmcnt(0)\n"
      "v_readfirstlane_b32 s55, v2\n"
      "s_cmp_ge_u32 s55, 2\n"
      "s_cbranch_scc1 .Lq_spe18\n"
      "s_sleep 1\n"
      "s_branch .Lq_spin17\n"
      ".Lq_spe18:\n"
      "s_mov_b32 s54, 262144\n"
      ".Lq_spin19:\n"
      "ds_read_b32 v2, v6 offset:1024\n"
      "s_sub_u32 s54, s54, 1\n"
      "s_cbranch_scc1 .Lq_spe20\n"
      "s_waitcnt lgkmcnt(0)\n"
      "v_readfirstlane_b32 s55, v2\n"
      "s_cmp_ge_u32 s55, 26\n"
      "s_cbranch_scc1 .Lq_spe20\n"
      "s_sleep 1\n"
      "s_branch .Lq_spin19\n"
      ".Lq_spe20:\n"
      "s_mov_b32 s54, 262144\n"
      ".Lq_s3rb:\n"
      "s_add_u32 s48, s58, 0\n"
      "buffer_load_dwordx4 v[16:19], v3, s[32:35], s48 offen offset:0 sc1\n"
      "s_add_u32 s49, s59, 0\n"
      "buffer_load_dwordx4 v[20:23], v3, s[32:35], s49 offen offset:0 sc1\n"
      "buffer_load_dwordx4 v[24:27], v3, s[32:35], s48 offen offset:1024 sc1\n"
      "buffer_load_dwordx4 v[28:31], v3, s[32:35], s49 offen offset:1024 sc1\n"
      "buffer_load_dwordx4 v[32:35], v3, s[32:35], s48 offen offset:2048 sc1\n"
      "buffer_load_dwordx4 v[36:39], v3, s[32:35], s49 offen offset:2048 sc1\n"
      "buffer_load_dwordx4 v[40:43], v3, s[32:35], s48 offen offset:3072 sc1\n"
      "buffer_load_dwordx4 v[44:47], v3, s[32:35], s49 offen offset:3072 sc1\n"
      "s_add_u32 s48, s58, 4096\n"
      "buffer_load_dwordx4 v[48:51], v3, s[32:35], s48 offen offset:0 sc1\n"
      "s_add_u32 s49, s59, 4096\n"
      "buffer_load_dwordx4 v[52:55], v3, s[32:35], s49 offen offset:0 sc1\n"
      "buffer_load_dwordx4 v[56:59], v3, s[32:35], s48 offen offset:1024 sc1\n"
      "buffer_load_dwordx4 v[60:63], v3, s[32:35], s49 offen offset:1024 sc1\n"
      "v_add_u32 v9, 0, v13\n"
      "v_add_u32 v10, 0, v14\n"
      "v_add_u32 v11, 0, v15\n"
      "v_add_u32 v8, 131072, v12\n"
      "ds_read_b64_tr_b16 v[156:157], v9 offset:0\n"
      "ds_read_b64_tr_b16 v[158:159], v9 offset:512\n"
      "ds_read_b64_tr_b16 v[160:161], v10 offset:0\n"
      "ds_read_b64_tr_b16 v[162:163], v10 offset:512\n"
      "ds_read_b64_tr_b16 v[164:165], v11 offset:0\n"
      "ds_read_b64_tr_b16 v[166:167], v11 offset:512\n"
      "s_waitcnt vmcnt(10)\n"
      "v_cmp_u_f32 vcc, v16, v20\n"
      "s_cbranch_vccnz .Lq_hs21\n"
      ".Lq_hb22:\n"
      "s_waitcnt lgkmcnt(2)\n"
      "v_mfma_f32_16x16x32_bf16 v[204:207], v[156:159], v[20:23], 0\n"
      "ds_read_b64_tr_b16 v[172:173], v9 offset:8192\n"
      "s_add_u32 s48, s58, 4096\n"
      "buffer_load_dwordx4 v[64:67], v3, s[32:35], s48 offen offset:2048 sc1\n"
      "v_mfma_f32_16x16x32_bf16 v[188:191], v[156:159], v[16:19], 0\n"
      "ds_read_b64_tr_b16 v[174:175], v9 offset:8704\n"
      "s_add_u32 s49, s59, 4096\n"
      "buffer_load_dwordx4 v[68:71], v3, s[32:35], s49 offen offset:2048 sc1\n"
      "v_mfma_f32_16x16x32_bf16 v[208:211], v[160:163], v[20:23], 0\n"
      "ds_read_b64_tr_b16 v[176:177], v10 offset:8192\n"
      "v_mfma_f32_16x16x32_bf16 v[192:195], v[160:163], v[16:19], 0\n"
      "ds_read_b64_tr_b16 v[178:179], v10 offset:8704\n"
      "s_waitcnt vmcnt(10) lgkmcnt(4)\n"
      "v_mfma_f32_16x16x32_bf16 v[212:215], v[164:167], v[20:23], 0\n"
      "v_cmp_u_f32 vcc, v24, v28\n"
      "ds_read_b64_tr_b16 v[180:181], v11 offset:8192\n"
      "v_mfma_f32_16x16x32_bf16 v[196:199], v[164:167], v[16:19], 0\n"
      "ds_read_b64_tr_b16 v[182:183], v11 offset:8704\n"
      "s_cbranch_vccnz .Lq_hs23\n"
      ".Lq_hb24:\n"
      "s_waitcnt lgkmcnt(2)\n"
      "v_mfma_f32_16x16x32_bf16 v[204:207], v[172:175], v[28:31], v[204:207]\n"
      "ds_read_b64_tr_b16 v[156:157], v9 offset:16384\n"
      "buffer_load_dwordx4 v[72:75], v3, s[32:35], s48 offen offset:3072 sc1\n"
      "v_mfma_f32_16x16x32_bf16 v[188:191], v[172:175], v[24:27], v[188:191]\n"
      "ds_read_b64_tr_b16 v[158:159], v9 offset:16896\n"
      "buffer_load_dwordx4 v[76:79], v3, s[32:35], s49 offen offset:3072 sc1\n"
      "v_mfma_f32_16x16x32_bf16 v[208:211], v[176:179], v[28:31], v[208:211]\n"
      "ds_read_b64_tr_b16 v[160:161], v10 offset:16384\n"
      "v_mfma_f32_16x16x32_bf16 v[192:195], v[176:179], v[24:27], v[192:195]\n"
      "ds_read_b64_tr_b16 v[162:163], v10 offset:16896\n"
      "s_waitcnt vmcnt(10) lgkmcnt(4)\n"
      "v_mfma_f32_16x16x32_bf16 v[212:215], v[180:183], v[28:31], v[212:215]\n"
      "v_cmp_u_f32 vcc, v32, v36\n"
      "ds_read_b64_tr_b16 v[164:165], v11 offset:16384\n"
      "v_mfma_f32_16x16x32_bf16 v[196:199], v[180:183], v[24:27], v[196:199]\n"
      "ds_read_b64_tr_b16 v[166:167], v11 offset:16896\n"
      "s_cbranch_vccnz .Lq_hs25\n"
      ".Lq_hb26:\n"
      "s_waitcnt lgkmcnt(2)\n"
      "v_mfma_f32_16x16x32_bf16 v[204:207], v[156:159], v[36:39], v[204:207]\n"
      "ds_read_b64_tr_b16 v[172:173], v9 offset:24576\n"
      "s_add_u32 s48, s58, 8192\n"
      "buffer_load_dwordx4 v[80:83], v3, s[32:35], s48 offen offset:0 sc1\n"
      "v_mfma_f32_16x16x32_bf16 v[188:191], v[156:159], v[32:35], v[188:191]\n"
      "ds_read_b64_tr_b16 v[174:175], v9 offset:25088\n"
      "s_add_u32 s49, s59, 8192\n"
      "buffer_load_dwordx4 v[84:87], v3, s[32:35], s49 offen offset:0 sc1\n"
      "v_mfma_f32_16x16x32_bf16 v[208:211], v[160:163], v[36:39], v[208:211]\n"
      "ds_read_b64_tr_b16 v[176:177], v10 offset:24576\n"
      "v_mfma_f32_16x16x32_bf16 v[192:195], v[160:163], v[32:35], v[192:195]\n"
      "ds_read_b64_tr_b16 v[178:179], v10 offset:25088\n"
      "s_waitcnt vmcnt(10) lgkmcnt(4)\n"
      "v_mfma_f32_16x16x32_bf16 v[212:215], v[164:167], v[36:39], v[212:215]\n"
      "v_cmp_u_f32 vcc, v40, v44\n"
      "ds_read_b64_tr_b16 v[180:181], v11 offset:24576\n"
      "v_mfma_f32_16x16x32_bf16 v[196:199], v[164:167], v[32:35], v[196:199]\n"
      "ds_read_b64_tr_b16 v[182:183], v11 offset:25088\n"
      "s_cbranch_vccnz .Lq_hs27\n"
      ".Lq_hb28:\n"
      "s_waitcnt lgkmcnt(2)\n"
      "v_mfma_f32_16x16x32_bf16 v[204:207], v[172:175], v[44:47], v[204:207]\n"
      "ds_read_b64_tr_b16 v[156:157], v9 offset:32768\n"
      "buffer_load_dwordx4 v[88:91], v3, s[32:35], s48 offen offset:1024 sc1\n"
      "v_mfma_f32_16x16x32_bf16 v[188:191], v[172:175], v[40:43], v[188:191]\n"
      "ds_read_b64_tr_b16 v[158:159], v9 offset:33280\n"
      "buffer_load_dwordx4 v[92:95], v3, s[32:35], s49 offen offset:1024 sc1\n"
      "v_mfma_f32_16x16x32_bf16 v[208:211], v[176:179], v[44:47], v[208:211]\n"
      "ds_read_b64_tr_b16 v[160:161], v10 offset:32768\n"
      "v_mfma_f32_16x16x32_bf16 v[192:195], v[176:179], v[40:43], v[192:195]\n"
      "ds_read_b64_tr_b16 v[162:163], v10 offset:33280\n"
      "s_waitcnt vmcnt(10) lgkmcnt(4)\n"
      "v_mfma_f32_16x16x32_bf16 v[212:215], v[180:183], v[44:47], v[212:215]\n"
      "v_cmp_u_f32 vcc, v48, v52\n"
      "ds_read_b64_tr_b16 v[164:165], v11 offset:32768\n"
      "v_mfma_f32_16x16x32_bf16 v[196:199], v[180:183], v[40:43], v[196:199]\n"
      "ds_read_b64_tr_b16 v[166:167], v11 offset:33280\n"
      "s_cbranch_vccnz .Lq_hs29\n"
      ".Lq_hb30:\n"
      "s_waitcnt lgkmcnt(2)\n"
      "v_mfma_f32_16x16x32_bf16 v[204:207], v[156:159], v[52:55], v[204:207]\n"
      "ds_read_b64_tr_b16 v[172:173], v9 offset:40960\n"
      "buffer_load_dwordx4 v[96:99], v3, s[32:35], s48 offen offset:2048 sc1\n"
      "v_mfma_f32_16x16x32_bf16 v[188:191], v[156:159], v[48:51], v[188:191]\n"
      "ds_read_b64_tr_b16 v[174:175], v9 offset:41472\n"
      "buffer_load_dwordx4 v[100:103], v3, s[32:35], s49 offen offset:2048 sc1\n"
      "v_mfma_f32_16x16x32_bf16 v[208:211], v[160:163], v[52:55], v[208:211]\n"
      "ds_read_b64_tr_b16 v[176:177], v10 offset:40960\n"
      "v_mfma_f32_16x16x32_bf16 v[192:195], v[160:163], v[48:51], v[192:195]\n"
      "ds_read_b64_tr_b16 v[178:179], v10 offset:41472\n"
      "s_waitcnt vmcnt(10) lgkmcnt(4)\n"
      "v_mfma_f32_16x16x32_bf16 v[212:215], v[164:167], v[52:55], v[212:215]\n"
      "v_cmp_u_f32 vcc, v56, v60\n"
      "ds_read_b64_tr_b16 v[180:181], v11 offset:40960\n"
      "v_mfma_f32_16x16x32_bf16 v[196:199], v[164:167], v[48:51], v[196:199]\n"
      "ds_read_b64_tr_b16 v[182:183], v11 offset:41472\n"
      "s_cbranch_vccnz .Lq_hs31\n"
      ".Lq_hb32:\n"
      "s_waitcnt lgkmcnt(2)\n"
      "v_mfma_f32_16x16x32_bf16 v[204:207], v[172:175], v[60:63], v[204:207]\n"
      "ds_read_b64_tr_b16 v[156:157], v9 offset:49152\n"
      "buffer_load_dwordx4 v[104:107], v3, s[32:35], s48 offen offset:3072 sc1\n"
      "v_mfma_f32_16x16x32_bf16 v[188:191], v[172:175], v[56:59], v[188:191]\n"
      "ds_read_b64_tr_b16 v[158:159], v9 offset:49664\n"
      "buffer_load_dwordx4 v[108:111], v3, s[32:35], s49 offen offset:3072 sc1\n"
      "v_mfma_f32_16x16x32_bf16 v[208:211], v[176:179], v[60:63], v[208:211]\n"
      "ds_read_b64_tr_b16 v[160:161], v10 offset:49152\n"
      "v_mfma_f32_16x16x32_bf16 v[192:195], v[176:179], v[56:59], v[192:195]\n"
      "ds_read_b64_tr_b16 v[162:163], v10 offset:49664\n"
      "s_waitcnt vmcnt(10) lgkmcnt(4)\n"
      "v_mfma_f32_16x16x32_bf16 v[212:215], v[180:183], v[60:63], v[212:215]\n"
      "v_cmp_u_f32 vcc, v64, v68\n"
      "ds_read_b64_tr_b16 v[164:165], v11 offset:49152\n"
      "v_mfma_f32_16x16x32_bf16 v[196:199], v[180:183], v[56:59], v[196:199]\n"
      "ds_read_b64_tr_b16 v[166:167], v11 offset:49664\n"
      "s_cbranch_vccnz .Lq_hs33\n"
      ".Lq_hb34:\n"
      "s_waitcnt lgkmcnt(2)\n"
      "v_mfma_f32_16x16x32_bf16 v[204:207], v[156:159], v[68:71], v[204:207]\n"
      "ds_read_b64_tr_b16 v[172:173], v9 offset:57344\n"
      "s_add_u32 s48, s58, 12288\n"
      "buffer_load_dwordx4 v[112:115], v3, s[32:35], s48 offen offset:0 sc1\n"
      "v_mfma_f32_16x16x32_bf16 v[188:191], v[156:159], v[64:67], v[188:191]\n"
      "ds_read_b64_tr_b16 v[174:175], v9 offset:57856\n"
      "s_add_u32 s49, s59, 12288\n"
      "buffer_load_dwordx4 v[116:119], v3, s[32:35], s49 offen offset:0 sc1\n"
      "v_mfma_f32_16x16x32_bf16 v[208:211], v[160:163], v[68:71], v[208:211]\n"
      "ds_read_b64_tr_b16 v[176:177], v10 offset:57344\n"
      "v_mfma_f32_16x16x32_bf16 v[192:195], v[160:163], v[64:67], v[192:195]\n"
      "ds_read_b64_tr_b16 v[178:179], v10 offset:57856\n"
      "s_waitcnt vmcnt(10) lgkmcnt(4)\n"
      "v_mfma_f32_16x16x32_bf16 v[212:215], v[164:167], v[68:71], v[212:215]\n"
      "v_cmp_u_f32 vcc, v72, v76\n"
      "ds_read_b64_tr_b16 v[180:181], v11 offset:57344\n"
      "v_mfma_f32_16x16x32_bf16 v[196:199], v[164:167], v[64:67], v[196:199]\n"
      "ds_read_b64_tr_b16 v[182:183], v11 offset:57856\n"
      "s_cbranch_vccnz .Lq_hs35\n"
      ".Lq_hb36:\n"
      ".Lq_s3a:\n"
      "s_waitcnt lgkmcnt(2)\n"
      "v_mfma_f32_16x16x32_bf16 v[204:207], v[172:175], v[76:79], v[204:207]\n"
      "v_add_u32 v9, 0x10000, v9\n"
      "ds_read_b64_tr_b16 v[156:157], v9 offset:0\n"
      "buffer_load_dwordx4 v[120:123], v3, s[32:35], s48 offen offset:1024 sc1\n"
      "v_mfma_f32_16x16x32_bf16 v[188:191], v[172:175], v[72:75], v[188:191]\n"
      "ds_read_b64_tr_b16 v[158:159], v9 offset:512\n"
      "buffer_load_dwordx4 v[124:127], v3, s[32:35], s49 offen offset:1024 sc1\n"
      "v_mfma_f32_16x16x32_bf16 v[208:211], v[176:179], v[76:79], v[208:211]\n"
      "v_add_u32 v10, 0x10000, v10\n"
      "ds_read_b64_tr_b16 v[160:161], v10 offset:0\n"
      "v_mfma_f32_16x16x32_bf16 v[192:195], v[176:179], v[72:75], v[192:195]\n"
      "ds_read_b64_tr_b16 v[162:163], v10 offset:512\n"
      "s_waitcnt vmcnt(10) lgkmcnt(4)\n"
      "v_mfma_f32_16x16x32_bf16 v[212:215], v[180:183], v[76:79], v[212:215]\n"
      "v_cmp_u_f32 vcc, v80, v84\n"
      "v_add_u32 v11, 0x10000, v11\n"
      "ds_read_b64_tr_b16 v[164:165], v11 offset:0\n"
      "v_mfma_f32_16x16x32_bf16 v[196:199], v[180:183], v[72:75], v[196:199]\n"
      "ds_read_b64_tr_b16 v[166:167], v11 offset:512\n"
      "s_cbranch_vccnz .Lq_hs37\n"
      ".Lq_hb38:\n"
      "s_waitcnt lgkmcnt(2)\n"
      "v_mfma_f32_16x16x32_bf16 v[204:207], v[156:159], v[84:87], v[204:207]\n"
      "ds_read_b64_tr_b16 v[172:173], v9 offset:8192\n"
      "buffer_load_dwordx4 v[128:131], v3, s[32:35], s48 offen offset:2048 sc1\n"
      "v_mfma_f32_16x16x32_bf16 v[188:191], v[156:159], v[80:83], v[188:191]\n"
      "ds_read_b64_tr_b16 v[174:175], v9 offset:8704\n"
      "buffer_load_dwordx4 v[132:135], v3, s[32:35], s49 offen offset:2048 sc1\n"
      "v_mfma_f32_16x16x32_bf16 v[208:211], v[160:163], v[84:87], v[208:211]\n"
      "ds_read_b64_tr_b16 v[176:177], v10 offset:8192\n"
      "v_mfma_f32_16x16x32_bf16 v[192:195], v[160:163], v[80:83], v[192:195]\n"
      "ds_read_b64_tr_b16 v[178:179], v10 offset:8704\n"
      "s_waitcnt vmcnt(10) lgkmcnt(4)\n"
      "v_mfma_f32_16x16x32_bf16 v[212:215], v[164:167], v[84:87], v[212:215]\n"
      "v_cmp_u_f32 vcc, v88, v92\n"
      "ds_read_b64_tr_b16 v[180:181], v11 offset:8192\n"
      "v_mfma_f32_16x16x32_bf16 v[196:199], v[164:167], v[80:83], v[196:199]\n"
      "ds_read_b64_tr_b16 v[182:183], v11 offset:8704\n"
      "s_cbranch_vccnz .Lq_hs39\n"
      ".Lq_hb40:\n"
      "s_waitcnt lgkmcnt(2)\n"
      "v_mfma_f32_16x16x32_bf16 v[204:207], v[172:175], v[92:95], v[204:207]\n"
      "ds_read_b64_tr_b16 v[156:157], v9 offset:16384\n"
      "buffer_load_dwordx4 v[136:139], v3, s[32:35], s48 offen offset:3072 sc1\n"
      "v_mfma_f32_16x16x32_bf16 v[188:191], v[172:175], v[88:91], v[188:191]\n"
      "ds_read_b64_tr_b16 v[158:159], v9 offset:16896\n"
      "buffer_load_dwordx4 v[140:143], v3, s[32:35], s49 offen offset:3072 sc1\n"
      "v_mfma_f32_16x16x32_bf16 v[208:211], v[176:179], v[92:95], v[208:211]\n"
      "ds_read_b64_tr_b16 v[160:161], v10 offset:16384\n"
      "v_mfma_f32_16x16x32_bf16 v[192:195], v[176:179], v[88:91], v[192:195]\n"
      "ds_read_b64_tr_b16 v[162:163], v10 offset:16896\n"
      "s_waitcnt vmcnt(10) lgkmcnt(4)\n"
      "v_mfma_f32_16x16x32_bf16 v[212:215], v[180:183], v[92:95], v[212:215]\n"
      "v_cmp_u_f32 vcc, v96, v100\n"
      "ds_read_b64_tr_b16 v[164:165], v11 offset:16384\n"
      "v_mfma_f32_16x16x32_bf16 v[196:199], v[180:183], v[88:91], v[196:199]\n"
      "ds_read_b64_tr_b16 v[166:167], v11 offset:16896\n"
      "s_cbranch_vccnz .Lq_hs41\n"
      ".Lq_hb42:\n"
      "s_waitcnt lgkmcnt(2)\n"
      "v_mfma_f32_16x16x32_bf16 v[204:207], v[156:159], v[100:103], v[204:207]\n"
      "ds_read_b64_tr_b16 v[172:173], v9 offset:24576\n"
      "ds_read_b32 v2, v6 offset:1024\n"
      "s_add_u32 s48, s58, 16384\n"
      "buffer_load_dwordx4 v[144:147], v3, s[32:35], s48 offen offset:0 sc1\n"
      "v_mfma_f32_16x16x32_bf16 v[188:191], v[156:159], v[96:99], v[188:191]\n"
      "ds_read_b64_tr_b16 v[174:175], v9 offset:25088\n"
      "s_add_u32 s49, s59, 16384\n"
      "buffer_load_dwordx4 v[148:151], v3, s[32:35], s49 offen offset:0 sc1\n"
      "v_mfma_f32_16x16x32_bf16 v[208:211], v[160:163], v[100:103], v[208:211]\n"
      "ds_read_b64_tr_b16 v[176:177], v10 offset:24576\n"
      "v_mfma_f32_16x16x32_bf16 v[192:195], v[160:163], v[96:99], v[192:195]\n"
      "ds_read_b64_tr_b16 v[178:179], v10 offset:25088\n"
      "s_waitcnt vmcnt(10) lgkmcnt(4)\n"
      "v_mfma_f32_16x16x32_bf16 v[212:215], v[164:167], v[100:103], v[212:215]\n"
      "v_cmp_u_f32 vcc, v104, v108\n"
      "ds_read_b64_tr_b16 v[180:181], v11 offset:24576\n"
      "v_mfma_f32_16x16x32_bf16 v[196:199], v[164:167], v[96:99], v[196:199]\n"
      "ds_read_b64_tr_b16 v[182:183], v11 offset:25088\n"
      "s_cbranch_vccnz .Lq_hs43\n"
      ".Lq_hb44:\n"
      "s_waitcnt lgkmcnt(2)\n"
      "v_mfma_f32_16x16x32_bf16 v[204:207], v[172:175], v[108:111], v[204:207]\n"
      "ds_read_b64_tr_b16 v[156:157], v9 offset:32768\n"
      "v_readfirstlane_b32 s55, v2\n"
      "v_mfma_f32_16x16x32_bf16 v[188:191], v[172:175], v[104:107], v[188:191]\n"
      "ds_read_b64_tr_b16 v[158:159], v9 offset:33280\n"
      "v_mfma_f32_16x16x32_bf16 v[208:211], v[176:179], v[108:111], v[208:211]\n"
      "ds_read_b64_tr_b16 v[160:161], v10 offset:32768\n"
      "s_cmp_ge_u32 s55, 28\n"
      "v_mfma_f32_16x16x32_bf16 v[192:195], v[176:179], v[104:107], v[192:195]\n"
      "ds_read_b64_tr_b16 v[162:163], v10 offset:33280\n"
      "s_cbranch_scc0 .Lq_vs47\n"
      ".Lq_vb48:\n"
      "s_waitcnt vmcnt(8) lgkmcnt(4)\n"
      "v_mfma_f32_16x16x32_bf16 v[212:215], v[180:183], v[108:111], v[212:215]\n"
      "v_cmp_u_f32 vcc, v112, v116\n"
      "ds_read_b64_tr_b16 v[164:165], v11 offset:32768\n"
      "v_mfma_f32_16x16x32_bf16 v[196:199], v[180:183], v[104:107], v[196:199]\n"
      "ds_read_b64_tr_b16 v[166:167], v11 offset:33280\n"
      "s_cbranch_vccnz .Lq_hs45\n"
      ".Lq_hb46:\n"
      "s_waitcnt lgkmcnt(2)\n"
      "v_mfma_f32_16x16x32_bf16 v[204:207], v[156:159], v[116:119], v[204:207]\n"
      "ds_read_b64_tr_b16 v[172:173], v9 offset:40960\n"
      "v_mfma_f32_16x16x32_bf16 v[188:191], v[156:159], v[112:115], v[188:191]\n"
      "ds_read_b64_tr_b16 v[174:175], v9 offset:41472\n"
      "v_mfma_f32_16x16x32_bf16 v[208:211], v[160:163], v[116:119], v[208:211]\n"
      "ds_read_b64_tr_b16 v[176:177], v10 offset:40960\n"
      "s_cmp_ge_u32 s55, 30\n"
      "v_mfma_f32_16x16x32_bf16 v[192:195], v[160:163], v[112:115], v[192:195]\n"
      "ds_read_b64_tr_b16 v[178:179], v10 offset:41472\n"
      "s_cbranch_scc0 .Lq_vs51\n"
      ".Lq_vb52:\n"
      "s_waitcnt vmcnt(6) lgkmcnt(4)\n"
      "v_mfma_f32_16x16x32_bf16 v[212:215], v[164:167], v[116:119], v[212:215]\n"
      "v_cmp_u_f32 vcc, v120, v124\n"
      "ds_read_b64_tr_b16 v[180:181], v11 offset:40960\n"
      "v_mfma_f32_16x16x32_bf16 v[196:199], v[164:167], v[112:115], v[196:199]\n"
      "ds_read_b64_tr_b16 v[182:183], v11 offset:41472\n"
      "s_cbranch_vccnz .Lq_hs49\n"
      ".Lq_hb50:\n"
      "s_waitcnt lgkmcnt(2)\n"
      "v_mfma_f32_16x16x32_bf16 v[204:207], v[172:175], v[124:127], v[204:207]\n"
      "ds_read_b64_tr_b16 v[156:157], v9 offset:49152\n"
      "v_mfma_f32_16x16x32_bf16 v[188:191], v[172:175], v[120:123], v[188:191]\n"
      "ds_read_b64_tr_b16 v[158:159], v9 offset:49664\n"
      "v_mfma_f32_16x16x32_bf16 v[208:211], v[176:179], v[124:127], v[208:211]\n"
      "ds_read_b64_tr_b16 v[160:161], v10 offset:49152\n"
      "s_cmp_ge_u32 s55, 32\n"
      "v_mfma_f32_16x16x32_bf16 v[192:195], v[176:179], v[120:123], v[192:195]\n"
      "ds_read_b64_tr_b16 v[162:163], v10 offset:49664\n"
      "s_cbranch_scc0 .Lq_vs55\n"
      ".Lq_vb56:\n"
      "s_waitcnt vmcnt(4) lgkmcnt(4)\n"
      "v_mfma_f32_16x16x32_bf16 v[212:215], v[180:183], v[124:127], v[212:215]\n"
      "v_cmp_u_f32 vcc, v128, v132\n"
      "ds_read_b64_tr_b16 v[164:165], v11 offset:49152\n"
      "v_mfma_f32_16x16x32_bf16 v[196:199], v[180:183], v[120:123], v[196:199]\n"
      "ds_read_b64_tr_b16 v[166:167], v11 offset:49664\n"
      "s_cbranch_vccnz .Lq_hs53\n"
      ".Lq_hb54:\n"
      "s_waitcnt lgkmcnt(2)\n"
      "v_mfma_f32_16x16x32_bf16 v[204:207], v[156:159], v[132:135], v[204:207]\n"
      "ds_read_b64_tr_b16 v[172:173], v9 offset:57344\n"
      "v_mfma_f32_16x16x32_bf16 v[188:191], v[156:159], v[128:131], v[188:191]\n"
      "ds_read_b64_tr_b16 v[174:175], v9 offset:57856\n"
      "v_mfma_f32_16x16x32_bf16 v[208:211], v[160:163], v[132:135], v[208:211]\n"
      "ds_read_b64_tr_b16 v[176:177], v10 offset:57344\n"
      "s_cmp_ge_u32 s55, 34\n"
      "v_mfma_f32_16x16x32_bf16 v[192:195], v[160:163], v[128:131], v[192:195]\n"
      "ds_read_b64_tr_b16 v[178:179], v10 offset:57856\n"
      "s_cbranch_scc0 .Lq_vs59\n"
      ".Lq_vb60:\n"
      "s_waitcnt vmcnt(2) lgkmcnt(4)\n"
      "v_mfma_f32_16x16x32_bf16 v[212:215], v[164:167], v[132:135], v[212:215]\n"
      "v_cmp_u_f32 vcc, v136, v140\n"
      "ds_read_b64_tr_b16 v[180:181], v11 offset:57344\n"
      "v_mfma_f32_16x16x32_bf16 v[196:199], v[164:167], v[128:131], v[196:199]\n"
      "ds_read_b64_tr_b16 v[182:183], v11 offset:57856\n"
      "s_cbranch_vccnz .Lq_hs57\n"
      ".Lq_hb58:\n"
      "s_waitcnt lgkmcnt(2)\n"
      "v_mfma_f32_16x16x32_bf16 v[204:207], v[172:175], v[140:143], v[204:207]\n"
      "v_add_u32 v9, 0x10000, v9\n"
      "ds_read_b64_tr_b16 v[156:157], v9 offset:0\n"
      "v_mfma_f32_16x16x32_bf16 v[188:191], v[172:175], v[136:139], v[188:191]\n"
      "ds_read_b64_tr_b16 v[158:159], v9 offset:512\n"
      "v_mfma_f32_16x16x32_bf16 v[208:211], v[176:179], v[140:143], v[208:211]\n"
      "v_add_u32 v10, 0x10000, v10\n"
      "ds_read_b64_tr_b16 v[160:161], v10 offset:0\n"
      "v_mfma_f32_16x16x32_bf16 v[192:195], v[176:179], v[136:139], v[192:195]\n"
      "ds_read_b64_tr_b16 v[162:163], v10 offset:512\n"
      "s_waitcnt vmcnt(0) lgkmcnt(4)\n"
      "v_mfma_f32_16x16x32_bf16 v[212:215], v[180:183], v[140:143], v[212:215]\n"
      "v_cmp_u_f32 vcc, v144, v148\n"
      "v_add_u32 v11, 0x10000, v11\n"
      "ds_read_b64_tr_b16 v[164:165], v11 offset:0\n"
      "v_mfma_f32_16x16x32_bf16 v[196:199], v[180:183], v[136:139], v[196:199]\n"
      "ds_read_b64_tr_b16 v[166:167], v11 offset:512\n"
      "s_cbranch_vccnz .Lq_hs61\n"
      ".Lq_hb62:\n"
      "s_waitcnt lgkmcnt(4)\n"
      "v_mfma_f32_16x16x32_bf16 v[204:207], v[156:159], v[148:151], v[204:207]\n"
      "buffer_store_dwordx2 v[222:223], v221, s[32:35], 0 offen sc1\n"
      "ds_read_b64_tr_b16 v[168:169], v8 offset:0\n"
      "ds_read_b64_tr_b16 v[170:171], v8 offset:512\n"
      "s_waitcnt lgkmcnt(4)\n"
      "v_mfma_f32_16x16x32_bf16 v[208:211], v[160:163], v[148:151], v[208:211]\n"
      "v_subrev_u32 v8, 0x10000, v8\n"
      "ds_read_b64_tr_b16 v[172:173], v8 offset:57344\n"
      "ds_read_b64_tr_b16 v[174:175], v8 offset:57856\n"
      "s_waitcnt lgkmcnt(4)\n"
      "v_mfma_f32_16x16x32_bf16 v[212:215], v[164:167], v[148:151], v[212:215]\n"
      "ds_read_b64_tr_b16 v[176:177], v8 offset:49152\n"
      "ds_read_b64_tr_b16 v[178:179], v8 offset:49664\n"
      "v_mfma_f32_16x16x32_bf16 v[188:191], v[156:159], v[144:147], v[188:191]\n"
      "ds_write_b128 v217, v[204:207] offset:1024\n"
      "ds_read_b64_tr_b16 v[180:181], v8 offset:40960\n"
      "ds_read_b64_tr_b16 v[182:183], v8 offset:41472\n"
      "v_mfma_f32_16x16x32_bf16 v[192:195], v[160:163], v[144:147], v[192:195]\n"
      "ds_write_b128 v217, v[208:211] offset:2048\n"
      "v_mfma_f32_16x16x32_bf16 v[196:199], v[164:167], v[144:147], v[196:199]\n"
      "ds_write_b128 v217, v[212:215] offset:3072\n"
      "v_mov_b32 v2, 100\n"
      "v_cmp_u_f32 vcc, v204, v188\n"
      "s_waitcnt lgkmcnt(8)\n"
      "s_cbranch_vccnz .Lq_nan\n"
      ".Lq_nanok:\n"
      "ds_write_b32 v7, v2\n"
      ".Lq_xr:\n"
      "ds_read_b32 v5, v219\n"
      "ds_read_b128 v[156:159], v218 offset:1024\n"
      "ds_read_b128 v[160:163], v218 offset:2048\n"
      "ds_read_b128 v[164:167], v218 offset:3072\n"
      "s_sub_u32 s54, s54, 1\n"
      "s_cbranch_scc1 .Lq_end\n"
      "s_waitcnt lgkmcnt(3)\n"
      "v_cmp_gt_u32 vcc, 100, v5\n"
      "s_cbranch_vccnz .Lq_xr\n"
      "s_waitcnt lgkmcnt(2)\n"
      "v_pk_add_f32 v[188:189], v[188:189], v[156:157]\n"
      "v_pk_add_f32 v[190:191], v[190:191], v[158:159]\n"
      "v_and_or_b32 v188, v188, v220, s12\n"
      "v_and_or_b32 v189, v189, v220, s13\n"
      "v_and_or_b32 v190, v190, v220, s14\n"
      "v_and_or_b32 v191, v191, v220, s15\n"
      "ds_read_b32 v2, v6 offset:1024\n"
      "buffer_store_dwordx4 v[188:191], v3, s[32:35], s5 offen offset:0 sc1\n"
      "s_waitcnt lgkmcnt(1)\n"
      "v_pk_add_f32 v[192:193], v[192:193], v[160:161]\n"
      "v_pk_add_f32 v[194:195], v[194:195], v[162:163]\n"
      "v_and_or_b32 v192, v192, v220, s12\n"
      "v_and_or_b32 v193, v193, v220, s13\n"
      "v_and_or_b32 v194, v194, v220, s14\n"
      "v_and_or_b32 v195, v195, v220, s15\n"
      "buffer_store_dwordx4 v[192:195], v3, s[32:35], s6 offen offset:1024 sc1\n"
      "s_waitcnt lgkmcnt(0)\n"
      "v_pk_add_f32 v[196:197], v[196:197], v[164:165]\n"
      "v_pk_add_f32 v[198:199], v[198:199], v[166:167]\n"
      "v_and_or_b32 v196, v196, v220, s12\n"
      "v_and_or_b32 v197, v197, v220, s13\n"
      "v_and_or_b32 v198, v198, v220, s14\n"
      "v_and_or_b32 v199, v199, v220, s15\n"
      "buffer_store_dwordx4 v[196:199], v3, s[32:35], s7 offen offset:2048 sc1\n"
      "v_readfirstlane_b32 s47, v2\n"
      "s_cmp_ge_u32 s47, 200\n"
      "s_cbranch_scc0 .Lq_y0\n"
      "buffer_load_dwordx4 v[224:227], v3, s[32:35], s4 offen offset:0 sc1\n"
      "buffer_load_dwordx4 v[228:231], v3, s[32:35], s4 offen offset:1024 sc1\n"
      "buffer_load_dwordx4 v[232:235], v3, s[32:35], s4 offen offset:2048 sc1\n"
      ".Lq_y0:\n"
      "s_bitcmp1_b32 s41, 0\n"
      "v_mfma_f32_16x16x32_bf16 v[200:203], v[168:171], v[148:151], 0\n"
      "ds_read_b64_tr_b16 v[152:153], v8 offset:32768\n"
      "ds_read_b64_tr_b16 v[154:155], v8 offset:33280\n"
      "v_mfma_f32_16x16x32_bf16 v[184:187], v[168:171], v[144:147], 0\n"
      "v_mfma_f32_16x16x32_bf16 v[200:203], v[172:175], v[140:143], v[200:203]\n"
      "ds_read_b64_tr_b16 v[156:157], v8 offset:24576\n"
      "ds_read_b64_tr_b16 v[158:159], v8 offset:25088\n"
      "v_mfma_f32_16x16x32_bf16 v[184:187], v[172:175], v[136:139], v[184:187]\n"
      "v_mfma_f32_16x16x32_bf16 v[200:203], v[176:179], v[132:135], v[200:203]\n"
      "ds_read_b64_tr_b16 v[160:161], v8 offset:16384\n"
      "ds_read_b64_tr_b16 v[162:163], v8 offset:16896\n"
      "v_mfma_f32_16x16x32_bf16 v[184:187], v[176:179], v[128:131], v[184:187]\n"
      "v_mfma_f32_16x16x32_bf16 v[200:203], v[180:183], v[124:127], v[200:203]\n"
      "ds_read_b64_tr_b16 v[164:165], v8 offset:8192\n"
      "ds_read_b64_tr_b16 v[166:167], v8 offset:8704\n"
      "v_mfma_f32_16x16x32_bf16 v[184:187], v[180:183], v[120:123], v[184:187]\n"
      "s_waitcnt lgkmcnt(6)\n"
      "v_mfma_f32_16x16x32_bf16 v[200:203], v[152:155], v[116:119], v[200:203]\n"
      "ds_read_b64_tr_b16 v[168:169], v8 offset:0\n"
      "ds_read_b64_tr_b16 v[170:171], v8 offset:512\n"
      "v_mfma_f32_16x16x32_bf16 v[184:187], v[152:155], v[112:115], v[184:187]\n"
      "s_waitcnt lgkmcnt(6)\n"
      "v_mfma_f32_16x16x32_bf16 v[200:203], v[156:159], v[108:111], v[200:203]\n"
      "v_subrev_u32 v8, 0x10000, v8\n"
      "ds_read_b64_tr_b16 v[172:173], v8 offset:57344\n"
      "ds_read_b64_tr_b16 v[174:175], v8 offset:57856\n"
      "v_mfma_f32_16x16x32_bf16 v[184:187], v[156:159], v[104:107], v[184:187]\n"
      "s_waitcnt lgkmcnt(6)\n"
      "v_mfma_f32_16x16x32_bf16 v[200:203], v[160:163], v[100:103], v[200:203]\n"
      "ds_read_b64_tr_b16 v[176:177], v8 offset:49152\n"
      "ds_read_b64_tr_b16 v[178:179], v8 offset:49664\n"
      "v_mfma_f32_16x16x32_bf16 v[184:187], v[160:163], v[96:99], v[184:187]\n"
      "s_waitcnt lgkmcnt(6)\n"
      "v_mfma_f32_16x16x32_bf16 v[200:203], v[164:167], v[92:95], v[200:203]\n"
      "ds_read_b64_tr_b16 v[180:181], v8 offset:40960\n"
      "ds_read_b64_tr_b16 v[182:183], v8 offset:41472\n"
      "v_mfma_f32_16x16x32_bf16 v[184:187], v[164:167], v[88:91], v[184:187]\n"
      "s_waitcnt lgkmcnt(6)\n"
      "v_mfma_f32_16x16x32_bf16 v[200:203], v[168:171], v[84:87], v[200:203]\n"
      "ds_read_b64_tr_b16 v[152:153], v8 offset:32768\n"
      "ds_read_b64_tr_b16 v[154:155], v8 offset:33280\n"
      "v_mfma_f32_16x16x32_bf16 v[184:187], v[168:171], v[80:83], v[184:187]\n"
      "s_waitcnt lgkmcnt(6)\n"
      "v_mfma_f32_16x16x32_bf16 v[200:203], v[172:175], v[76:79], v[200:203]\n"
      "ds_read_b64_tr_b16 v[156:157], v8 offset:24576\n"
      "ds_read_b64_tr_b16 v[158:159], v8 offset:25088\n"
      "v_mfma_f32_16x16x32_bf16 v[184:187], v[172:175], v[72:75], v[184:187]\n"
      "s_waitcnt lgkmcnt(6)\n"
      "v_mfma_f32_16x16x32_bf16 v[200:203], v[176:179], v[68:71], v[200:203]\n"
      "ds_read_b64_tr_b16 v[160:161], v8 offset:16384\n"
      "ds_read_b64_tr_b16 v[162:163], v8 offset:16896\n"
      "v_mfma_f32_16x16x32_bf16 v[184:187], v[176:179], v[64:67], v[184:187]\n"
      "s_cbranch_scc0 .Lq_y1\n"
      "s_waitcnt lgkmcnt(6)\n"
      "v_mfma_f32_16x16x32_bf16 v[200:203], v[180:183], v[60:63], v[200:203]\n"
      "ds_read_b64_tr_b16 v[164:165], v8 offset:8192\n"
      "ds_read_b64_tr_b16 v[166:167], v8 offset:8704\n"
      "v_mfma_f32_16x16x32_bf16 v[184:187], v[180:183], v[56:59], v[184:187]\n"
      "s_waitcnt lgkmcnt(6)\n"
      "v_mfma_f32_16x16x32_bf16 v[200:203], v[152:155], v[52:55], v[200:203]\n"
      "ds_read_b64_tr_b16 v[168:169], v8 offset:0\n"
      "ds_read_b64_tr_b16 v[170:171], v8 offset:512\n"
      "v_mfma_f32_16x16x32_bf16 v[184:187], v[152:155], v[48:51], v[184:187]\n"
      "s_waitcnt lgkmcnt(6)\n"
      "v_mfma_f32_16x16x32_bf16 v[200:203], v[156:159], v[44:47], v[200:203]\n"
      "v_mfma_f32_16x16x32_bf16 v[184:187], v[156:159], v[40:43], v[184:187]\n"
      "s_waitcnt lgkmcnt(4)\n"
      "v_mfma_f32_16x16x32_bf16 v[200:203], v[160:163], v[36:39], v[200:203]\n"
      "v_mfma_f32_16x16x32_bf16 v[184:187], v[160:163], v[32:35], v[184:187]\n"
      "s_waitcnt lgkmcnt(2)\n"
      "v_mfma_f32_16x16x32_bf16 v[200:203], v[164:167], v[28:31], v[200:203]\n"
      "v_mfma_f32_16x16x32_bf16 v[184:187], v[164:167], v[24:27], v[184:187]\n"
      "s_waitcnt lgkmcnt(0)\n"
      "v_mfma_f32_16x16x32_bf16 v[200:203], v[168:171], v[20:23], v[200:203]\n"
      "v_mfma_f32_16x16x32_bf16 v[184:187], v[168:171], v[16:19], v[184:187]\n"
      ".Lq_y1:\n"
      "v_mov_b32 v2, 101\n"
      "s_nop 5\n"
      "ds_write_b128 v217, v[200:203]\n"
      "ds_write_b32 v7, v2\n"
      ".Lq_xr2:\n"
      "ds_read_b32 v5, v219\n"
      "ds_read_b128 v[204:207], v218\n"
      "s_sub_u32 s54, s54, 1\n"
      "s_cbranch_scc1 .Lq_end\n"
      "s_waitcnt lgkmcnt(0)\n"
      "v_cmp_gt_u32 vcc, 101, v5\n"
      "s_cbranch_vccnz .Lq_xr2\n"
      "v_pk_add_f32 v[184:185], v[184:185], v[204:205]\n"
      "v_pk_add_f32 v[186:187], v[186:187], v[206:207]\n"
      "s_cmp_ge_u32 s47, 200\n"
      "s_cbranch_scc1 .Lq_pl1\n"
      "s_mov_b32 s54, 262144\n"
      ".Lq_spin63:\n"
      "ds_read_b32 v2, v6 offset:1024\n"
      "s_sub_u32 s54, s54, 1\n"
      "s_cbranch_scc1 .Lq_spe64\n"
      "s_waitcnt lgkmcnt(0)\n"
      "v_readfirstlane_b32 s55, v2\n"
      "s_cmp_ge_u32 s55, 200\n"
      "s_cbranch_scc0 .Lq_spin63\n"
      ".Lq_spe64:\n"
      "s_mov_b32 s54, 262144\n"
      ".Lq_pl:\n"
      "buffer_load_dwordx4 v[224:227], v3, s[32:35], s4 offen offset:0 sc1\n"
      "buffer_load_dwordx4 v[228:231], v3, s[32:35], s4 offen offset:1024 sc1\n"
      "buffer_load_dwordx4 v[232:235], v3, s[32:35], s4 offen offset:2048 sc1\n"
      ".Lq_pl1:\n"
      "s_sub_u32 s54, s54, 1\n"
      "s_cbranch_scc1 .Lq_ple\n"
      "s_waitcnt vmcnt(2)\n"
      "v_xor_b32 v224, s12, v224\n"
      "v_xor_b32 v225, s13, v225\n"
      "v_xor_b32 v226, s14, v226\n"
      "v_xor_b32 v227, s15, v227\n"
      "v_or3_b32 v2, v224, v225, v226\n"
      "v_or_b32 v2, v2, v227\n"
      "s_waitcnt vmcnt(1)\n"
      "v_xor_b32 v228, s12, v228\n"
      "v_xor_b32 v229, s13, v229\n"
      "v_xor_b32 v230, s14, v230\n"
      "v_xor_b32 v231, s15, v231\n"
      "v_or3_b32 v2, v228, v229, v2\n"
      "v_or3_b32 v2, v230, v231, v2\n"
      "s_waitcnt vmcnt(0)\n"
      "v_xor_b32 v232, s12, v232\n"
      "v_xor_b32 v233, s13, v233\n"
      "v_xor_b32 v234, s14, v234\n"
      "v_xor_b32 v235, s15, v235\n"
      "v_or3_b32 v2, v232, v233, v2\n"
      "v_or3_b32 v2, v234, v235, v2\n"
      "v_and_b32 v2, 0xff, v2\n"
      "v_cmp_ne_u32 vcc, 0, v2\n"
      "s_cbranch_vccnz .Lq_pl\n"
      ".Lq_ple:\n"
      "s_waitcnt vmcnt(0)\n"
      "v_pk_add_f32 v[184:185], v[184:185], v[224:225]\n"
      "v_pk_add_f32 v[228:229], v[228:229], v[232:233]\n"
      "v_pk_add_f32 v[186:187], v[186:187], v[226:227]\n"
      "v_pk_add_f32 v[230:231], v[230:231], v[234:235]\n"
      "v_pk_add_f32 v[184:185], v[184:185], v[228:229]\n"
      "v_pk_add_f32 v[186:187], v[186:187], v[230:231]\n"
      "v_cvt_pk_bf16_f32 v4, v184, v185\n"
      "v_cvt_pk_bf16_f32 v5, v186, v187\n"
      "buffer_store_dwordx2 v[4:5], v216, s[28:31], s8 offen\n"
      ".Lq_end:\n"
      "s_endpgm\n"
      ".Lq_vs47:\n"
      "ds_read_b32 v2, v6 offset:1024\n"
      "s_sub_u32 s54, s54, 1\n"
      "s_cbranch_scc1 .Lq_end\n"
      "s_waitcnt lgkmcnt(0)\n"
      "v_readfirstlane_b32 s55, v2\n"
      "s_cmp_ge_u32 s55, 28\n"
      "s_cbranch_scc0 .Lq_vs47\n"
      "s_branch .Lq_vb48\n"
      ".Lq_vs51:\n"
      "ds_read_b32 v2, v6 offset:1024\n"
      "s_sub_u32 s54, s54, 1\n"
      "s_cbranch_scc1 .Lq_end\n"
      "s_waitcnt lgkmcnt(0)\n"
      "v_readfirstlane_b32 s55, v2\n"
      "s_cmp_ge_u32 s55, 30\n"
      "s_cbranch_scc0 .Lq_vs51\n"
      "s_branch .Lq_vb52\n"
      ".Lq_vs55:\n"
      "ds_read_b32 v2, v6 offset:1024\n"
      "s_sub_u32 s54, s54, 1\n"
      "s_cbranch_scc1 .Lq_end\n"
      "s_waitcnt lgkmcnt(0)\n"
      "v_readfirstlane_b32 s55, v2\n"
      "s_cmp_ge_u32 s55, 32\n"
      "s_cbranch_scc0 .Lq_vs55\n"
      "s_branch .Lq_vb56\n"
      ".Lq_vs59:\n"
      "ds_read_b32 v2, v6 offset:1024\n"
      "s_sub_u32 s54, s54, 1\n"
      "s_cbranch_scc1 .Lq_end\n"
      "s_waitcnt lgkmcnt(0)\n"
      "v_readfirstlane_b32 s55, v2\n"
      "s_cmp_ge_u32 s55, 34\n"
      "s_cbranch_scc0 .Lq_vs59\n"
      "s_branch .Lq_vb60\n"
      ".Lq_hs7:\n"
      "s_add_u32 s50, s58, 6144\n"
      "buffer_load_dwordx4 v[64:67], v3, s[32:35], s50 offen sc1\n"
      "s_add_u32 s50, s59, 6144\n"
      "buffer_load_dwordx4 v[68:71], v3, s[32:35], s50 offen sc1\n"
      "s_sub_u32 s54, s54, 1\n"
      "s_cbranch_scc1 .Lq_end\n"
      "s_waitcnt vmcnt(0)\n"
      "v_max_u32 v4, v64, v68\n"
      "v_cmp_eq_u32 vcc, -1, v4\n"
      "s_cbranch_vccnz .Lq_hs7\n"
      "s_branch .Lq_hb8\n"
      ".Lq_hs9:\n"
      "s_add_u32 s50, s58, 7168\n"
      "buffer_load_dwordx4 v[72:75], v3, s[32:35], s50 offen sc1\n"
      "s_add_u32 s50, s59, 7168\n"
      "buffer_load_dwordx4 v[76:79], v3, s[32:35], s50 offen sc1\n"
      "s_sub_u32 s54, s54, 1\n"
      "s_cbranch_scc1 .Lq_end\n"
      "s_waitcnt vmcnt(0)\n"
      "v_max_u32 v4, v72, v76\n"
      "v_cmp_eq_u32 vcc, -1, v4\n"
      "s_cbranch_vccnz .Lq_hs9\n"
      "s_branch .Lq_hb10\n"
      ".Lq_hs21:\n"
      "s_add_u32 s50, s58, 0\n"
      "buffer_load_dwordx4 v[16:19], v3, s[32:35], s50 offen sc1\n"
      "s_add_u32 s50, s59, 0\n"
      "buffer_load_dwordx4 v[20:23], v3, s[32:35], s50 offen sc1\n"
      "s_sub_u32 s54, s54, 1\n"
      "s_cbranch_scc1 .Lq_end\n"
      "s_waitcnt vmcnt(0)\n"
      "v_max_u32 v4, v16, v20\n"
      "v_cmp_eq_u32 vcc, -1, v4\n"
      "s_cbranch_vccnz .Lq_hs21\n"
      "s_branch .Lq_hb22\n"
      ".Lq_hs23:\n"
      "s_add_u32 s50, s58, 1024\n"
      "buffer_load_dwordx4 v[24:27], v3, s[32:35], s50 offen sc1\n"
      "s_add_u32 s50, s59, 1024\n"
      "buffer_load_dwordx4 v[28:31], v3, s[32:35], s50 offen sc1\n"
      "s_sub_u32 s54, s54, 1\n"
      "s_cbranch_scc1 .Lq_end\n"
      "s_waitcnt vmcnt(0)\n"
      "v_max_u32 v4, v24, v28\n"
      "v_cmp_eq_u32 vcc, -1, v4\n"
      "s_cbranch_vccnz .Lq_hs23\n"
      "s_branch .Lq_hb24\n"
      ".Lq_hs25:\n"
      "s_add_u32 s50, s58, 2048\n"
      "buffer_load_dwordx4 v[32:35], v3, s[32:35], s50 offen sc1\n"
      "s_add_u32 s50, s59, 2048\n"
      "buffer_load_dwordx4 v[36:39], v3, s[32:35], s50 offen sc1\n"
      "s_sub_u32 s54, s54, 1\n"
      "s_cbranch_scc1 .Lq_end\n"
      "s_waitcnt vmcnt(0)\n"
      "v_max_u32 v4, v32, v36\n"
      "v_cmp_eq_u32 vcc, -1, v4\n"
      "s_cbranch_vccnz .Lq_hs25\n"
      "s_branch .Lq_hb26\n"
      ".Lq_hs27:\n"
      "s_add_u32 s50, s58, 3072\n"
      "buffer_load_dwordx4 v[40:43], v3, s[32:35], s50 offen sc1\n"
      "s_add_u32 s50, s59, 3072\n"
      "buffer_load_dwordx4 v[44:47], v3, s[32:35], s50 offen sc1\n"
      "s_sub_u32 s54, s54, 1\n"
      "s_cbranch_scc1 .Lq_end\n"
      "s_waitcnt vmcnt(0)\n"
      "v_max_u32 v4, v40, v44\n"
      "v_cmp_eq_u32 vcc, -1, v4\n"
      "s_cbranch_vccnz .Lq_hs27\n"
      "s_branch .Lq_hb28\n"
      ".Lq_hs29:\n"
      "s_add_u32 s50, s58, 4096\n"
      "buffer_load_dwordx4 v[48:51], v3, s[32:35], s50 offen sc1\n"
      "s_add_u32 s50, s59, 4096\n"
      "buffer_load_dwordx4 v[52:55], v3, s[32:35], s50 offen sc1\n"
      "s_sub_u32 s54, s54, 1\n"
      "s_cbranch_scc1 .Lq_end\n"
      "s_waitcnt vmcnt(0)\n"
      "v_max_u32 v4, v48, v52\n"
      "v_cmp_eq_u32 vcc, -1, v4\n"
      "s_cbranch_vccnz .Lq_hs29\n"
      "s_branch .Lq_hb30\n"
      ".Lq_hs31:\n"
      "s_add_u32 s50, s58, 5120\n"
      "buffer_load_dwordx4 v[56:59], v3, s[32:35], s50 offen sc1\n"
      "s_add_u32 s50, s59, 5120\n"
      "buffer_load_dwordx4 v[60:63], v3, s[32:35], s50 offen sc1\n"
      "s_sub_u32 s54, s54, 1\n"
      "s_cbranch_scc1 .Lq_end\n"
      "s_waitcnt vmcnt(0)\n"
      "v_max_u32 v4, v56, v60\n"
      "v_cmp_eq_u32 vcc, -1, v4\n"
      "s_cbranch_vccnz .Lq_hs31\n"
      "s_branch .Lq_hb32\n"
      ".Lq_hs33:\n"
      "s_add_u32 s50, s58, 6144\n"
      "buffer_load_dwordx4 v[64:67], v3, s[32:35], s50 offen sc1\n"
      "s_add_u32 s50, s59, 6144\n"
      "buffer_load_dwordx4 v[68:71], v3, s[32:35], s50 offen sc1\n"
      "s_sub_u32 s54, s54, 1\n"
      "s_cbranch_scc1 .Lq_end\n"
      "s_waitcnt vmcnt(0)\n"
      "v_max_u32 v4, v64, v68\n"
      "v_cmp_eq_u32 vcc, -1, v4\n"
      "s_cbranch_vccnz .Lq_hs33\n"
      "s_branch .Lq_hb34\n"
      ".Lq_hs35:\n"
      "s_add_u32 s50, s58, 7168\n"
      "buffer_load_dwordx4 v[72:75], v3, s[32:35], s50 offen sc1\n"
      "s_add_u32 s50, s59, 7168\n"
      "buffer_load_dwordx4 v[76:79], v3, s[32:35], s50 offen sc1\n"
      "s_sub_u32 s54, s54, 1\n"
      "s_cbranch_scc1 .Lq_end\n"
      "s_waitcnt vmcnt(0)\n"
      "v_max_u32 v4, v72, v76\n"
      "v_cmp_eq_u32 vcc, -1, v4\n"
      "s_cbranch_vccnz .Lq_hs35\n"
      "s_branch .Lq_hb36\n"
      ".Lq_hs37:\n"
      "s_add_u32 s50, s58, 8192\n"
      "buffer_load_dwordx4 v[80:83], v3, s[32:35], s50 offen sc1\n"
      "s_add_u32 s50, s59, 8192\n"
      "buffer_load_dwordx4 v[84:87], v3, s[32:35], s50 offen sc1\n"
      "s_sub_u32 s54, s54, 1\n"
      "s_cbranch_scc1 .Lq_end\n"
      "s_waitcnt vmcnt(0)\n"
      "v_max_u32 v4, v80, v84\n"
      "v_cmp_eq_u32 vcc, -1, v4\n"
      "s_cbranch_vccnz .Lq_hs37\n"
      "s_branch .Lq_hb38\n"
      ".Lq_hs39:\n"
      "s_add_u32 s50, s58, 9216\n"
      "buffer_load_dwordx4 v[88:91], v3, s[32:35], s50 offen sc1\n"
      "s_add_u32 s50, s59, 9216\n"
      "buffer_load_dwordx4 v[92:95], v3, s[32:35], s50 offen sc1\n"
      "s_sub_u32 s54, s54, 1\n"
      "s_cbranch_scc1 .Lq_end\n"
      "s_waitcnt vmcnt(0)\n"
      "v_max_u32 v4, v88, v92\n"
      "v_cmp_eq_u32 vcc, -1, v4\n"
      "s_cbranch_vccnz .Lq_hs39\n"
      "s_branch .Lq_hb40\n"
      ".Lq_hs41:\n"
      "s_add_u32 s50, s58, 10240\n"
      "buffer_load_dwordx4 v[96:99], v3, s[32:35], s50 offen sc1\n"
      "s_add_u32 s50, s59, 10240\n"
      "buffer_load_dwordx4 v[100:103], v3, s[32:35], s50 offen sc1\n"
      "s_sub_u32 s54, s54, 1\n"
      "s_cbranch_scc1 .Lq_end\n"
      "s_waitcnt vmcnt(0)\n"
      "v_max_u32 v4, v96, v100\n"
      "v_cmp_eq_u32 vcc, -1, v4\n"
      "s_cbranch_vccnz .Lq_hs41\n"
      "s_branch .Lq_hb42\n"
      ".Lq_hs43:\n"
      "s_add_u32 s50, s58, 11264\n"
      "buffer_load_dwordx4 v[104:107], v3, s[32:35], s50 offen sc1\n"
      "s_add_u32 s50, s59, 11264\n"
      "buffer_load_dwordx4 v[108:111], v3, s[32:35], s50 offen sc1\n"
      "s_sub_u32 s54, s54, 1\n"
      "s_cbranch_scc1 .Lq_end\n"
      "s_waitcnt vmcnt(0)\n"
      "v_max_u32 v4, v104, v108\n"
      "v_cmp_eq_u32 vcc, -1, v4\n"
      "s_cbranch_vccnz .Lq_hs43\n"
      "s_branch .Lq_hb44\n"
      ".Lq_hs45:\n"
      "s_add_u32 s50, s58, 12288\n"
      "buffer_load_dwordx4 v[112:115], v3, s[32:35], s50 offen sc1\n"
      "s_add_u32 s50, s59, 12288\n"
      "buffer_load_dwordx4 v[116:119], v3, s[32:35], s50 offen sc1\n"
      "s_sub_u32 s54, s54, 1\n"
      "s_cbranch_scc1 .Lq_end\n"
      "s_waitcnt vmcnt(0)\n"
      "v_max_u32 v4, v112, v116\n"
      "v_cmp_eq_u32 vcc, -1, v4\n"
      "s_cbranch_vccnz .Lq_hs45\n"
      "s_branch .Lq_hb46\n"
      ".Lq_hs49:\n"
      "s_add_u32 s50, s58, 13312\n"
      "buffer_load_dwordx4 v[120:123], v3, s[32:35], s50 offen sc1\n"
      "s_add_u32 s50, s59, 13312\n"
      "buffer_load_dwordx4 v[124:127], v3, s[32:35], s50 offen sc1\n"
      "s_sub_u32 s54, s54, 1\n"
      "s_cbranch_scc1 .Lq_end\n"
      "s_waitcnt vmcnt(0)\n"
      "v_max_u32 v4, v120, v124\n"
      "v_cmp_eq_u32 vcc, -1, v4\n"
      "s_cbranch_vccnz .Lq_hs49\n"
      "s_branch .Lq_hb50\n"
      ".Lq_hs53:\n"
      "s_add_u32 s50, s58, 14336\n"
      "buffer_load_dwordx4 v[128:131], v3, s[32:35], s50 offen sc1\n"
      "s_add_u32 s50, s59, 14336\n"
      "buffer_load_dwordx4 v[132:135], v3, s[32:35], s50 offen sc1\n"
      "s_sub_u32 s54, s54, 1\n"
      "s_cbranch_scc1 .Lq_end\n"
      "s_waitcnt vmcnt(0)\n"
      "v_max_u32 v4, v128, v132\n"
      "v_cmp_eq_u32 vcc, -1, v4\n"
      "s_cbranch_vccnz .Lq_hs53\n"
      "s_branch .Lq_hb54\n"
      ".Lq_hs57:\n"
      "s_add_u32 s50, s58, 15360\n"
      "buffer_load_dwordx4 v[136:139], v3, s[32:35], s50 offen sc1\n"
      "s_add_u32 s50, s59, 15360\n"
      "buffer_load_dwordx4 v[140:143], v3, s[32:35], s50 offen sc1\n"
      "s_sub_u32 s54, s54, 1\n"
      "s_cbranch_scc1 .Lq_end\n"
      "s_waitcnt vmcnt(0)\n"
      "v_max_u32 v4, v136, v140\n"
      "v_cmp_eq_u32 vcc, -1, v4\n"
      "s_cbranch_vccnz .Lq_hs57\n"
      "s_branch .Lq_hb58\n"
      ".Lq_hs61:\n"
      "s_add_u32 s50, s58, 16384\n"
      "buffer_load_dwordx4 v[144:147], v3, s[32:35], s50 offen sc1\n"
      "s_add_u32 s50, s59, 16384\n"
      "buffer_load_dwordx4 v[148:151], v3, s[32:35], s50 offen sc1\n"
      "s_sub_u32 s54, s54, 1\n"
      "s_cbranch_scc1 .Lq_end\n"
      "s_waitcnt vmcnt(0)\n"
      "v_max_u32 v4, v144, v148\n"
      "v_cmp_eq_u32 vcc, -1, v4\n"
      "s_cbranch_vccnz .Lq_hs61\n"
      "s_branch .Lq_hb62\n"
      ".Lq_nan:\n"
      "s_mov_b32 s60, 0\n"
      ".Lq_hf0:\n"
      "s_sub_u32 s54, s54, 1\n"
      "s_cbranch_scc1 .Lq_end\n"
      "s_mov_b32 s47, 0\n"
      "s_waitcnt vmcnt(0)\n"
      "s_bitcmp1_b32 s41, 0\n"
      "s_cbranch_scc0 .Lq_hfa\n"
      "v_max3_u32 v2, v16, v17, v18\n"
      "v_max_u32 v2, v2, v19\n"
      "v_cmp_eq_u32 vcc, -1, v2\n"
      "s_cbranch_vccz .Lq_hf65\n"
      "s_add_u32 s47, s58, 0\n"
      "buffer_load_dwordx4 v[16:19], v3, s[32:35], s47 offen sc1\n"
      ".Lq_hf65:\n"
      "v_max3_u32 v2, v20, v21, v22\n"
      "v_max_u32 v2, v2, v23\n"
      "v_cmp_eq_u32 vcc, -1, v2\n"
      "s_cbranch_vccz .Lq_hf66\n"
      "s_add_u32 s47, s59, 0\n"
      "buffer_load_dwordx4 v[20:23], v3, s[32:35], s47 offen sc1\n"
      ".Lq_hf66:\n"
      "v_max3_u32 v2, v24, v25, v26\n"
      "v_max_u32 v2, v2, v27\n"
      "v_cmp_eq_u32 vcc, -1, v2\n"
      "s_cbranch_vccz .Lq_hf67\n"
      "s_add_u32 s47, s58, 1024\n"
      "buffer_load_dwordx4 v[24:27], v3, s[32:35], s47 offen sc1\n"
      ".Lq_hf67:\n"
      "v_max3_u32 v2, v28, v29, v30\n"
      "v_max_u32 v2, v2, v31\n"
      "v_cmp_eq_u32 vcc, -1, v2\n"
      "s_cbranch_vccz .Lq_hf68\n"
      "s_add_u32 s47, s59, 1024\n"
      "buffer_load_dwordx4 v[28:31], v3, s[32:35], s47 offen sc1\n"
      ".Lq_hf68:\n"
      "v_max3_u32 v2, v32, v33, v34\n"
      "v_max_u32 v2, v2, v35\n"
      "v_cmp_eq_u32 vcc, -1, v2\n"
      "s_cbranch_vccz .Lq_hf69\n"
      "s_add_u32 s47, s58, 2048\n"
      "buffer_load_dwordx4 v[32:35], v3, s[32:35], s47 offen sc1\n"
      ".Lq_hf69:\n"
      "v_max3_u32 v2, v36, v37, v38\n"
      "v_max_u32 v2, v2, v39\n"
      "v_cmp_eq_u32 vcc, -1, v2\n"
      "s_cbranch_vccz .Lq_hf70\n"
      "s_add_u32 s47, s59, 2048\n"
      "buffer_load_dwordx4 v[36:39], v3, s[32:35], s47 offen sc1\n"
      ".Lq_hf70:\n"
      "v_max3_u32 v2, v40, v41, v42\n"
      "v_max_u32 v2, v2, v43\n"
      "v_cmp_eq_u32 vcc, -1, v2\n"
      "s_cbranch_vccz .Lq_hf71\n"
      "s_add_u32 s47, s58, 3072\n"
      "buffer_load_dwordx4 v[40:43], v3, s[32:35], s47 offen sc1\n"
      ".Lq_hf71:\n"
      "v_max3_u32 v2, v44, v45, v46\n"
      "v_max_u32 v2, v2, v47\n"
      "v_cmp_eq_u32 vcc, -1, v2\n"
      "s_cbranch_vccz .Lq_hf72\n"
      "s_add_u32 s47, s59, 3072\n"
      "buffer_load_dwordx4 v[44:47], v3, s[32:35], s47 offen sc1\n"
      ".Lq_hf72:\n"
      "v_max3_u32 v2, v48, v49, v50\n"
      "v_max_u32 v2, v2, v51\n"
      "v_cmp_eq_u32 vcc, -1, v2\n"
      "s_cbranch_vccz .Lq_hf73\n"
      "s_add_u32 s47, s58, 4096\n"
      "buffer_load_dwordx4 v[48:51], v3, s[32:35], s47 offen sc1\n"
      ".Lq_hf73:\n"
      "v_max3_u32 v2, v52, v53, v54\n"
      "v_max_u32 v2, v2, v55\n"
      "v_cmp_eq_u32 vcc, -1, v2\n"
      "s_cbranch_vccz .Lq_hf74\n"
      "s_add_u32 s47, s59, 4096\n"
      "buffer_load_dwordx4 v[52:55], v3, s[32:35], s47 offen sc1\n"
      ".Lq_hf74:\n"
      "v_max3_u32 v2, v56, v57, v58\n"
      "v_max_u32 v2, v2, v59\n"
      "v_cmp_eq_u32 vcc, -1, v2\n"
      "s_cbranch_vccz .Lq_hf75\n"
      "s_add_u32 s47, s58, 5120\n"
      "buffer_load_dwordx4 v[56:59], v3, s[32:35], s47 offen sc1\n"
      ".Lq_hf75:\n"
      "v_max3_u32 v2, v60, v61, v62\n"
      "v_max_u32 v2, v2, v63\n"
      "v_cmp_eq_u32 vcc, -1, v2\n"
      "s_cbranch_vccz .Lq_hf76\n"
      "s_add_u32 s47, s59, 5120\n"
      "buffer_load_dwordx4 v[60:63], v3, s[32:35], s47 offen sc1\n"
      ".Lq_hf76:\n"
      ".Lq_hfa:\n"
      "v_max3_u32 v2, v64, v65, v66\n"
      "v_max_u32 v2, v2, v67\n"
      "v_cmp_eq_u32 vcc, -1, v2\n"
      "s_cbranch_vccz .Lq_hf77\n"
      "s_add_u32 s47, s58, 6144\n"
      "buffer_load_dwordx4 v[64:67], v3, s[32:35], s47 offen sc1\n"
      ".Lq_hf77:\n"
      "v_max3_u32 v2, v68, v69, v70\n"
      "v_max_u32 v2, v2, v71\n"
      "v_cmp_eq_u32 vcc, -1, v2\n"
      "s_cbranch_vccz .Lq_hf78\n"
      "s_add_u32 s47, s59, 6144\n"
      "buffer_load_dwordx4 v[68:71], v3, s[32:35], s47 offen sc1\n"
      ".Lq_hf78:\n"
      "v_max3_u32 v2, v72, v73, v74\n"
      "v_max_u32 v2, v2, v75\n"
      "v_cmp_eq_u32 vcc, -1, v2\n"
      "s_cbranch_vccz .Lq_hf79\n"
      "s_add_u32 s47, s58, 7168\n"
      "buffer_load_dwordx4 v[72:75], v3, s[32:35], s47 offen sc1\n"
      ".Lq_hf79:\n"
      "v_max3_u32 v2, v76, v77, v78\n"
      "v_max_u32 v2, v2, v79\n"
      "v_cmp_eq_u32 vcc, -1, v2\n"
      "s_cbranch_vccz .Lq_hf80\n"
      "s_add_u32 s47, s59, 7168\n"
      "buffer_load_dwordx4 v[76:79], v3, s[32:35], s47 offen sc1\n"
      ".Lq_hf80:\n"
      "v_max3_u32 v2, v80, v81, v82\n"
      "v_max_u32 v2, v2, v83\n"
      "v_cmp_eq_u32 vcc, -1, v2\n"
      "s_cbranch_vccz .Lq_hf81\n"
      "s_add_u32 s47, s58, 8192\n"
      "buffer_load_dwordx4 v[80:83], v3, s[32:35], s47 offen sc1\n"
      ".Lq_hf81:\n"
      "v_max3_u32 v2, v84, v85, v86\n"
      "v_max_u32 v2, v2, v87\n"
      "v_cmp_eq_u32 vcc, -1, v2\n"
      "s_cbranch_vccz .Lq_hf82\n"
      "s_add_u32 s47, s59, 8192\n"
      "buffer_load_dwordx4 v[84:87], v3, s[32:35], s47 offen sc1\n"
      ".Lq_hf82:\n"
      "v_max3_u32 v2, v88, v89, v90\n"
      "v_max_u32 v2, v2, v91\n"
      "v_cmp_eq_u32 vcc, -1, v2\n"
      "s_cbranch_vccz .Lq_hf83\n"
      "s_add_u32 s47, s58, 9216\n"
      "buffer_load_dwordx4 v[88:91], v3, s[32:35], s47 offen sc1\n"
      ".Lq_hf83:\n"
      "v_max3_u32 v2, v92, v93, v94\n"
      "v_max_u32 v2, v2, v95\n"
      "v_cmp_eq_u32 vcc, -1, v2\n"
      "s_cbranch_vccz .Lq_hf84\n"
      "s_add_u32 s47, s59, 9216\n"
      "buffer_load_dwordx4 v[92:95], v3, s[32:35], s47 offen sc1\n"
      ".Lq_hf84:\n"
      "v_max3_u32 v2, v96, v97, v98\n"
      "v_max_u32 v2, v2, v99\n"
      "v_cmp_eq_u32 vcc, -1, v2\n"
      "s_cbranch_vccz .Lq_hf85\n"
      "s_add_u32 s47, s58, 10240\n"
      "buffer_load_dwordx4 v[96:99], v3, s[32:35], s47 offen sc1\n"
      ".Lq_hf85:\n"
      "v_max3_u32 v2, v100, v101, v102\n"
      "v_max_u32 v2, v2, v103\n"
      "v_cmp_eq_u32 vcc, -1, v2\n"
      "s_cbranch_vccz .Lq_hf86\n"
      "s_add_u32 s47, s59, 10240\n"
      "buffer_load_dwordx4 v[100:103], v3, s[32:35], s47 offen sc1\n"
      ".Lq_hf86:\n"
      "v_max3_u32 v2, v104, v105, v106\n"
      "v_max_u32 v2, v2, v107\n"
      "v_cmp_eq_u32 vcc, -1, v2\n"
      "s_cbranch_vccz .Lq_hf87\n"
      "s_add_u32 s47, s58, 11264\n"
      "buffer_load_dwordx4 v[104:107], v3, s[32:35], s47 offen sc1\n"
      ".Lq_hf87:\n"
      "v_max3_u32 v2, v108, v109, v110\n"
      "v_max_u32 v2, v2, v111\n"
      "v_cmp_eq_u32 vcc, -1, v2\n"
      "s_cbranch_vccz .Lq_hf88\n"
      "s_add_u32 s47, s59, 11264\n"
      "buffer_load_dwordx4 v[108:111], v3, s[32:35], s47 offen sc1\n"
      ".Lq_hf88:\n"
      "v_max3_u32 v2, v112, v113, v114\n"
      "v_max_u32 v2, v2, v115\n"
      "v_cmp_eq_u32 vcc, -1, v2\n"
      "s_cbranch_vccz .Lq_hf89\n"
      "s_add_u32 s47, s58, 12288\n"
      "buffer_load_dwordx4 v[112:115], v3, s[32:35], s47 offen sc1\n"
      ".Lq_hf89:\n"
      "v_max3_u32 v2, v116, v117, v118\n"
      "v_max_u32 v2, v2, v119\n"
      "v_cmp_eq_u32 vcc, -1, v2\n"
      "s_cbranch_vccz .Lq_hf90\n"
      "s_add_u32 s47, s59, 12288\n"
      "buffer_load_dwordx4 v[116:119], v3, s[32:35], s47 offen sc1\n"
      ".Lq_hf90:\n"
      "v_max3_u32 v2, v120, v121, v122\n"
      "v_max_u32 v2, v2, v123\n"
      "v_cmp_eq_u32 vcc, -1, v2\n"
      "s_cbranch_vccz .Lq_hf91\n"
      "s_add_u32 s47, s58, 13312\n"
      "buffer_load_dwordx4 v[120:123], v3, s[32:35], s47 offen sc1\n"
      ".Lq_hf91:\n"
      "v_max3_u32 v2, v124, v125, v126\n"
      "v_max_u32 v2, v2, v127\n"
      "v_cmp_eq_u32 vcc, -1, v2\n"
      "s_cbranch_vccz .Lq_hf92\n"
      "s_add_u32 s47, s59, 13312\n"
      "buffer_load_dwordx4 v[124:127], v3, s[32:35], s47 offen sc1\n"
      ".Lq_hf92:\n"
      "v_max3_u32 v2, v128, v129, v130\n"
      "v_max_u32 v2, v2, v131\n"
      "v_cmp_eq_u32 vcc, -1, v2\n"
      "s_cbranch_vccz .Lq_hf93\n"
      "s_add_u32 s47, s58, 14336\n"
      "buffer_load_dwordx4 v[128:131], v3, s[32:35], s47 offen sc1\n"
      ".Lq_hf93:\n"
      "v_max3_u32 v2, v132, v133, v134\n"
      "v_max_u32 v2, v2, v135\n"
      "v_cmp_eq_u32 vcc, -1, v2\n"
      "s_cbranch_vccz .Lq_hf94\n"
      "s_add_u32 s47, s59, 14336\n"
      "buffer_load_dwordx4 v[132:135], v3, s[32:35], s47 offen sc1\n"
      ".Lq_hf94:\n"
      "v_max3_u32 v2, v136, v137, v138\n"
      "v_max_u32 v2, v2, v139\n"
      "v_cmp_eq_u32 vcc, -1, v2\n"
      "s_cbranch_vccz .Lq_hf95\n"
      "s_add_u32 s47, s58, 15360\n"
      "buffer_load_dwordx4 v[136:139], v3, s[32:35], s47 offen sc1\n"
      ".Lq_hf95:\n"
      "v_max3_u32 v2, v140, v141, v142\n"
      "v_max_u32 v2, v2, v143\n"
      "v_cmp_eq_u32 vcc, -1, v2\n"
      "s_cbranch_vccz .Lq_hf96\n"
      "s_add_u32 s47, s59, 15360\n"
      "buffer_load_dwordx4 v[140:143], v3, s[32:35], s47 offen sc1\n"
      ".Lq_hf96:\n"
      "v_max3_u32 v2, v144, v145, v146\n"
      "v_max_u32 v2, v2, v147\n"
      "v_cmp_eq_u32 vcc, -1, v2\n"
      "s_cbranch_vccz .Lq_hf97\n"
      "s_add_u32 s47, s58, 16384\n"
      "buffer_load_dwordx4 v[144:147], v3, s[32:35], s47 offen sc1\n"
      ".Lq_hf97:\n"
      "v_max3_u32 v2, v148, v149, v150\n"
      "v_max_u32 v2, v2, v151\n"
      "v_cmp_eq_u32 vcc, -1, v2\n"
      "s_cbranch_vccz .Lq_hf98\n"
      "s_add_u32 s47, s59, 16384\n"
      "buffer_load_dwordx4 v[148:151], v3, s[32:35], s47 offen sc1\n"
      ".Lq_hf98:\n"
      "s_or_b32 s60, s60, s47\n"
      "s_cmp_lg_u32 s47, 0\n"
      "s_cbranch_scc1 .Lq_hf0\n"
      "v_mov_b32 v2, 100\n"
      "s_cmp_eq_u32 s60, 0\n"
      "s_cbranch_scc1 .Lq_nanok\n"
      "s_bitcmp1_b32 s41, 0\n"
      "s_cbranch_scc1 .Lq_s3rb\n"
      "s_branch .Lq_s3ra\n"
      ".Lq_xf0:\n"
      "buffer_load_dwordx4 v[16:19], v176, s[32:35], s62 offen sc1\n"
      "buffer_load_dwordx4 v[20:23], v177, s[32:35], s62 offen sc1\n"
      "s_branch .Lq_xfe\n"
      ".Lq_xf1:\n"
      "buffer_load_dwordx4 v[24:27], v178, s[32:35], s62 offen sc1\n"
      "buffer_load_dwordx4 v[28:31], v179, s[32:35], s62 offen sc1\n"
      "s_branch .Lq_xfe\n"
      ".Lq_xf2:\n"
      "buffer_load_dwordx4 v[32:35], v176, s[32:35], s62 offen sc1\n"
      "buffer_load_dwordx4 v[36:39], v177, s[32:35], s62 offen sc1\n"
      "s_branch .Lq_xfe\n"
      ".Lq_xf3:\n"
      "buffer_load_dwordx4 v[40:43], v178, s[32:35], s62 offen sc1\n"
      "buffer_load_dwordx4 v[44:47], v179, s[32:35], s62 offen sc1\n"
      "s_branch .Lq_xfe\n"
      ".Lq_xf4:\n"
      "buffer_load_dwordx4 v[48:51], v176, s[32:35], s62 offen sc1\n"
      "buffer_load_dwordx4 v[52:55], v177, s[32:35], s62 offen sc1\n"
      "s_branch .Lq_xfe\n"
      ".Lq_xf5:\n"
      "buffer_load_dwordx4 v[56:59], v178, s[32:35], s62 offen sc1\n"
      "buffer_load_dwordx4 v[60:63], v179, s[32:35], s62 offen sc1\n"
      "s_branch .Lq_xfe\n"
      ".Lq_xf6:\n"
      "buffer_load_dwordx4 v[64:67], v176, s[32:35], s62 offen sc1\n"
      "buffer_load_dwordx4 v[68:71], v177, s[32:35], s62 offen sc1\n"
      "s_branch .Lq_xfe\n"
      ".Lq_xf7:\n"
      "buffer_load_dwordx4 v[72:75], v178, s[32:35], s62 offen sc1\n"
      "buffer_load_dwordx4 v[76:79], v179, s[32:35], s62 offen sc1\n"
      ".Lq_xfe:\n"
      "s_sub_u32 s54, s54, 1\n"
      "s_cbranch_scc1 .Lq_end\n"
      "s_waitcnt vmcnt(0)\n"
      "s_setpc_b64 s[56:57]\n"
      ".Lq_bmain:\n"
      "s_setprio 1\n"
      "s_waitcnt vmcnt(0)\n"
      "s_lshr_b32 s41, s37, 2\n"
      "s_mul_i32 s43, s41, 56\n"
      "s_lshl_b32 s41, s41, 1\n"
      "s_add_u32 s41, s41, 1\n"
      "s_movk_i32 s53, 17\n"
      "s_branch .Lq_s3\n"
      ".Lq_bload2:\n"
      "s_cmp_lt_u32 s38, 28\n"
      "s_cbranch_scc1 .Lq_bl3\n"
      "s_and_b32 s40, s38, 1\n"
      ".Lq_bl3:\n"
      "s_waitcnt lgkmcnt(0)\n"
      "s_and_b32 s50, s44, 0x7f7f7f7f\n"
      "s_add_u32 s50, s50, 0x01010101\n"
      "s_and_b32 s51, s44, 0x80808080\n"
      "s_xor_b32 s44, s50, s51\n"
      "s_and_b32 s50, s45, 0x7f7f7f7f\n"
      "s_add_u32 s50, s50, 0x01010101\n"
      "s_and_b32 s51, s45, 0x80808080\n"
      "s_xor_b32 s45, s50, s51\n"
      "v_mov_b32 v12, s44\n"
      "v_mov_b32 v13, s45\n"
      ".Lq_bload3:\n"
      "ds_write_b32 v7, v12 offset:512\n"
      "ds_write_b32 v7, v13 offset:768\n"
      "s_mov_b32 s56, 0x65432180\n"
      "s_mov_b32 s57, 0xdcba97\n"
      "s_lshl_b32 s50, s40, 2\n"
      "s_lshr_b64 s[56:57], s[56:57], s50\n"
      "s_and_b32 s42, s56, 15\n"
      "s_and_b32 s51, s37, 3\n"
      "s_mul_i32 s51, s51, 14\n"
      "s_lshr_b32 s50, s24, 7\n"
      "s_add_u32 s50, s50, s51\n"
      "s_sub_u32 s50, 3, s50\n"
      "s_and_b32 s50, s50, 7\n"
      "s_add_u32 s42, s42, s50\n"
      "s_cmp_ge_u32 s42, 14\n"
      "s_cselect_b32 s50, 14, 0\n"
      "s_sub_u32 s42, s42, s50\n"
      "s_add_u32 s42, s42, s51\n"
      "v_bfe_u32 v1, v0, 3, 1\n"
      "v_lshrrev_b32 v4, 4, v0\n"
      "v_lshl_add_u32 v1, v1, 2, v4\n"
      "v_mul_u32_u24 v1, 7168, v1\n"
      "v_and_b32 v2, 0x70, v3\n"
      "v_xor_b32 v4, 0, v2\n"
      "v_add_u32 v4, v4, v1\n"
      "v_add_u32 v8, 0, v4\n"
      "v_xor_b32 v4, 32, v2\n"
      "v_add_u32 v4, v4, v1\n"
      "v_add_u32 v9, 56320, v4\n"
      "v_xor_b32 v4, 64, v2\n"
      "v_add_u32 v4, v4, v1\n"
      "v_add_u32 v10, 112640, v4\n"
      "v_xor_b32 v4, 96, v2\n"
      "v_add_u32 v4, v4, v1\n"
      "v_add_u32 v11, 168960, v4\n"
      "s_cmp_lt_u32 s38, 24\n"
      "s_cbranch_scc1 .Lq_bl5\n"
      "s_sleep 16\n"
      "s_lshl_b32 s52, s37, 8\n"
      "s_bfe_u32 s50, s38, 0x10001\n"
      "s_lshl_b32 s50, s50, 7\n"
      "s_add_u32 s52, s52, s50\n"
      "s_add_u32 s52, s52, 8192\n"
      "s_mov_b32 s53, 4\n"
      ".Lq_f1s:\n"
      "s_load_dwordx2 s[46:47], s[32:33], s52 glc\n"
      "s_waitcnt lgkmcnt(0)\n"
      "s_cmp_eq_u64 s[46:47], s[44:45]\n"
      "s_cbranch_scc1 .Lq_f1e\n"
      "s_sub_u32 s53, s53, 1\n"
      "s_cbranch_scc0 .Lq_f1s\n"
      "v_and_b32 v4, 1, v0\n"
      "v_lshlrev_b32 v4, 7, v4\n"
      "s_lshl_b32 s50, s37, 8\n"
      "s_add_u32 s50, s50, 8192\n"
      "s_mov_b32 s54, 262144\n"
      ".Lq_fp99:\n"
      "buffer_load_dwordx2 v[16:17], v4, s[32:35], s50 offen sc1\n"
      "s_sub_u32 s54, s54, 1\n"
      "s_cbranch_scc1 .Lq_fpe100\n"
      "s_waitcnt vmcnt(0)\n"
      "v_cmp_ne_u64 vcc, s[44:45], v[16:17]\n"
      "s_cbranch_vccz .Lq_fpe100\n"
      "s_sleep 2\n"
      "s_branch .Lq_fp99\n"
      ".Lq_fpe100:\n"
      ".Lq_f1e:\n"
      ".Lq_bl5:\n"
      "s_lshr_b32 s52, s37, 2\n"
      "s_mul_i32 s52, s52, 12845056\n"
      "s_lshl_b32 s50, s42, 7\n"
      "s_add_u32 s52, s52, s50\n"
      "s_mov_b32 m0, 0\n"
      "s_mov_b32 s53, 0\n"
      "s_lshl_b32 s51, s2, 4\n"
      "s_add_u32 s51, s51, 4096\n"
      "s_cmp_lt_u32 s38, 24\n"
      "s_waitcnt lgkmcnt(0)\n"
      "s_barrier\n"
      "s_setprio 0\n"
      "s_cbranch_scc1 .Lq_bl6\n"
      "s_barrier\n"
      ".Lq_bl6:\n"
      "s_mov_b64 exec, 1\n"
      "buffer_store_dwordx2 v[12:13], v0, s[32:35], s51 offen sc1\n"
      "s_mov_b64 exec, -1\n"
      "s_sub_u32 s50, s38, 16\n"
      "s_cmp_lt_u32 s50, 8\n"
      "s_cbranch_scc0 .Lq_bl7\n"
      "s_sleep 10\n"
      ".Lq_bl7:\n"
      "v_lshrrev_b32 v1, 4, v0\n"
      "v_and_b32 v2, 15, v0\n"
      "v_lshl_add_u32 v1, v1, 8, v2\n"
      "v_mul_u32_u24 v1, 14336, v1\n"
      "s_mul_i32 s46, s37, 14\n"
      "s_lshr_b32 s50, s8, 7\n"
      "s_add_u32 s50, s50, s46\n"
      "s_sub_u32 s50, 3, s50\n"
      "s_and_b32 s47, s50, 7\n"
      "s_sub_u32 s48, 23, s38\n"
      "s_add_u32 s50, s38, 56\n"
      "s_cmp_lt_u32 s38, 24\n"
      "s_cselect_b32 s48, s48, s50\n"
      "s_cselect_b32 s58, 10, 8\n"
      "s_movk_i32 s59, 96\n"
      "s_cselect_b32 s59, 80, s59\n"
      "s_branch .Lq_qh4\n"
      ".Lq_qh:\n"
      "s_cmp_lt_u32 s48, 12\n"
      "s_cbranch_scc0 .Lq_qh1\n"
      "s_and_b32 s50, s48, 1\n"
      "s_lshl_b32 s50, s50, 3\n"
      "s_add_u32 s50, s50, s47\n"
      "s_lshr_b32 s51, s48, 1\n"
      "s_add_u32 s51, s51, 4\n"
      "s_cmp_lt_u32 s50, 14\n"
      "s_cbranch_scc1 .Lq_qh2\n"
      "s_branch .Lq_qh3\n"
      ".Lq_qh1:\n"
      "s_sub_u32 s50, s48, 12\n"
      "s_mul_i32 s51, s50, 4682\n"
      "s_lshr_b32 s51, s51, 16\n"
      "s_mul_i32 s49, s51, 14\n"
      "s_sub_u32 s50, s50, s49\n"
      "s_add_u32 s51, s51, 10\n"
      ".Lq_qh2:\n"
      "s_lshr_b32 s49, s51, 1\n"
      "s_xor_b32 s49, s49, s51\n"
      "s_and_b32 s49, s49, 1\n"
      "s_mul_i32 s49, s49, 3\n"
      "s_xor_b32 s51, s51, s49\n"
      "s_add_u32 s50, s50, s46\n"
      "s_lshl_b32 s50, s50, 7\n"
      "s_mul_i32 s51, s51, 229376\n"
      "s_add_u32 s50, s50, s51\n"
      "buffer_load_dword v20, v1, s[8:11], s50 offen\n"
      "s_waitcnt vmcnt(3)\n"
      ".Lq_qh3:\n"
      "s_add_u32 s48, s48, s58\n"
      ".Lq_qh4:\n"
      "s_cmp_lt_u32 s48, s59\n"
      "s_cbranch_scc1 .Lq_qh\n"
      ".Lq_bl4:\n"
      "s_cmp_lt_u32 s38, 28\n"
      "s_cbranch_scc0 .Lq_spare\n"
      "v_mov_b32 v5, -6\n"
      "s_mov_b32 s53, 0xffffffef\n"
      ".Lq_vl101:\n"
      "v_max_i32 v2, 0, v5\n"
      "s_waitcnt vmcnt(31)\n"
      "buffer_load_dwordx4 v8, s[24:27], s52 offen offset:0 nt lds\n"
      "s_waitcnt vmcnt(31)\n"
      "buffer_load_dwordx4 v9, s[24:27], s52 offen offset:1024 nt lds\n"
      "s_waitcnt vmcnt(31)\n"
      "buffer_load_dwordx4 v10, s[24:27], s52 offen offset:2048 nt lds\n"
      "s_waitcnt vmcnt(31)\n"
      "buffer_load_dwordx4 v11, s[24:27], s52 offen offset:3072 nt lds\n"
      "s_add_u32 m0, m0, 0x1000\n"
      "s_add_u32 s52, s52, 3899392\n"
      "s_waitcnt vmcnt(31)\n"
      "buffer_load_dwordx4 v8, s[24:27], s52 offen offset:0 nt lds\n"
      "s_waitcnt vmcnt(31)\n"
      "buffer_load_dwordx4 v9, s[24:27], s52 offen offset:1024 nt lds\n"
      "s_waitcnt vmcnt(31)\n"
      "buffer_load_dwordx4 v10, s[24:27], s52 offen offset:2048 nt lds\n"
      "s_waitcnt vmcnt(31)\n"
      "ds_write_b32 v7, v2\n"
      "buffer_load_dwordx4 v11, s[24:27], s52 offen offset:3072 nt lds\n"
      "s_add_u32 m0, m0, 0x1000\n"
      "s_sub_u32 s52, s52, 3670016\n"
      "v_add_u32 v5, 2, v5\n"
      "s_add_u32 s53, s53, 1\n"
      "s_cbranch_scc0 .Lq_vl101\n"
      "s_branch .Lq_vtail\n"
      ".Lq_aload2:\n"
      "s_mov_b32 s56, 0x432180dc\n"
      "s_mov_b32 s57, 0xba9765\n"
      "s_lshl_b32 s50, s40, 2\n"
      "s_lshr_b64 s[56:57], s[56:57], s50\n"
      "s_and_b32 s39, s56, 15\n"
      "s_mul_i32 s51, s37, 14\n"
      "s_lshr_b32 s50, s8, 7\n"
      "s_add_u32 s50, s50, s51\n"
      "s_sub_u32 s50, 3, s50\n"
      "s_and_b32 s50, s50, 7\n"
      "s_add_u32 s39, s39, s50\n"
      "s_cmp_ge_u32 s39, 14\n"
      "s_cselect_b32 s50, 14, 0\n"
      "s_sub_u32 s39, s39, s50\n"
      "s_add_u32 s39, s39, s51\n"
      "s_lshr_b32 s50, s37, 2\n"
      "s_mul_i32 s51, s50, 56\n"
      "s_sub_u32 s39, s39, s51\n"
      "s_lshl_b32 s51, s51, 8\n"
      "s_lshl_b32 s50, s39, 2\n"
      "s_add_u32 s51, s51, s50\n"
      "s_add_u32 s61, s51, 16384\n"
      "v_lshlrev_b32 v14, 8, v0\n"
      "s_mov_b32 s56, 0x65432180\n"
      "s_mov_b32 s57, 0xdcba97\n"
      "s_lshl_b32 s50, s40, 2\n"
      "s_lshr_b64 s[56:57], s[56:57], s50\n"
      "s_and_b32 s42, s56, 15\n"
      "s_and_b32 s51, s37, 3\n"
      "s_mul_i32 s51, s51, 14\n"
      "s_lshr_b32 s50, s24, 7\n"
      "s_add_u32 s50, s50, s51\n"
      "s_sub_u32 s50, 3, s50\n"
      "s_and_b32 s50, s50, 7\n"
      "s_add_u32 s42, s42, s50\n"
      "s_cmp_ge_u32 s42, 14\n"
      "s_cselect_b32 s50, 14, 0\n"
      "s_sub_u32 s42, s42, s50\n"
      "s_add_u32 s42, s42, s51\n"
      "v_bfe_u32 v1, v0, 3, 1\n"
      "v_lshrrev_b32 v4, 4, v0\n"
      "v_lshl_add_u32 v1, v1, 2, v4\n"
      "v_mul_u32_u24 v1, 7168, v1\n"
      "v_and_b32 v2, 0x70, v3\n"
      "v_xor_b32 v4, 0, v2\n"
      "v_add_u32 v4, v4, v1\n"
      "v_add_u32 v8, 0, v4\n"
      "v_xor_b32 v4, 32, v2\n"
      "v_add_u32 v4, v4, v1\n"
      "v_add_u32 v9, 56320, v4\n"
      "v_xor_b32 v4, 64, v2\n"
      "v_add_u32 v4, v4, v1\n"
      "v_add_u32 v10, 112640, v4\n"
      "v_xor_b32 v4, 96, v2\n"
      "v_add_u32 v4, v4, v1\n"
      "v_add_u32 v11, 168960, v4\n"
      "s_waitcnt lgkmcnt(0)\n"
      "s_and_b32 s50, s44, 0x7f7f7f7f\n"
      "s_add_u32 s50, s50, 0x01010101\n"
      "s_and_b32 s51, s44, 0x80808080\n"
      "s_xor_b32 s44, s50, s51\n"
      "s_and_b32 s50, s45, 0x7f7f7f7f\n"
      "s_add_u32 s50, s50, 0x01010101\n"
      "s_and_b32 s51, s45, 0x80808080\n"
      "s_xor_b32 s45, s50, s51\n"
      "v_mov_b32 v12, s44\n"
      "v_mov_b32 v13, s45\n"
      "ds_write_b32 v7, v12 offset:512\n"
      "ds_write_b32 v7, v13 offset:768\n"
      "s_lshl_b32 s51, s2, 4\n"
      "s_add_u32 s51, s51, 4096\n"
      "s_mov_b64 exec, 1\n"
      "buffer_store_dwordx2 v[12:13], v0, s[32:35], s51 offen sc1\n"
      "s_mov_b64 exec, -1\n"
      "s_mov_b32 s54, 262144\n"
      ".Lq_spin102:\n"
      "ds_read_b32 v2, v6 offset:0\n"
      "s_sub_u32 s54, s54, 1\n"
      "s_cbranch_scc1 .Lq_spe103\n"
      "s_waitcnt lgkmcnt(0)\n"
      "v_readfirstlane_b32 s55, v2\n"
      "s_cmp_ge_u32 s55, 6\n"
      "s_cbranch_scc1 .Lq_spe103\n"
      "s_sleep 2\n"
      "s_branch .Lq_spin102\n"
      ".Lq_spe103:\n"
      "s_load_dwordx2 s[4:5], s[32:33], 0x1700\n"
      "s_load_dwordx2 s[12:13], s[32:33], 0x1740\n"
      "s_load_dwordx2 s[64:65], s[32:33], 0x1780\n"
      "s_load_dwordx2 s[72:73], s[32:33], 0x17c0\n"
      "s_mov_b64 vcc, 0\n"
      "s_waitcnt lgkmcnt(0)\n"
      "s_load_dwordx2 s[6:7], s[32:33], 0x1710\n"
      "s_load_dwordx2 s[8:9], s[32:33], 0x1720\n"
      "s_load_dwordx2 s[10:11], s[32:33], 0x1730\n"
      "s_load_dwordx2 s[14:15], s[32:33], 0x1750\n"
      "s_load_dwordx2 s[16:17], s[32:33], 0x1760\n"
      "s_load_dwordx2 s[18:19], s[32:33], 0x1770\n"
      "s_waitcnt lgkmcnt(0)\n"
      "s_load_dwordx2 s[66:67], s[32:33], 0x1790\n"
      "s_load_dwordx2 s[68:69], s[32:33], 0x17a0\n"
      "s_load_dwordx2 s[70:71], s[32:33], 0x17b0\n"
      "s_load_dwordx2 s[74:75], s[32:33], 0x17d0\n"
      "s_load_dwordx2 s[76:77], s[32:33], 0x17e0\n"
      "s_load_dwordx2 s[78:79], s[32:33], 0x17f0\n"
      "s_waitcnt lgkmcnt(0)\n"
      "s_xor_b64 s[4:5], s[4:5], s[44:45]\n"
      "s_or_b64 vcc, vcc, s[4:5]\n"
      "s_xor_b64 s[6:7], s[6:7], s[44:45]\n"
      "s_or_b64 vcc, vcc, s[6:7]\n"
      "s_xor_b64 s[8:9], s[8:9], s[44:45]\n"
      "s_or_b64 vcc, vcc, s[8:9]\n"
      "s_xor_b64 s[10:11], s[10:11], s[44:45]\n"
      "s_or_b64 vcc, vcc, s[10:11]\n"
      "s_xor_b64 s[12:13], s[12:13], s[44:45]\n"
      "s_or_b64 vcc, vcc, s[12:13]\n"
      "s_xor_b64 s[14:15], s[14:15], s[44:45]\n"
      "s_or_b64 vcc, vcc, s[14:15]\n"
      "s_xor_b64 s[16:17], s[16:17], s[44:45]\n"
      "s_or_b64 vcc, vcc, s[16:17]\n"
      "s_xor_b64 s[18:19], s[18:19], s[44:45]\n"
      "s_or_b64 vcc, vcc, s[18:19]\n"
      "s_xor_b64 s[64:65], s[64:65], s[44:45]\n"
      "s_or_b64 vcc, vcc, s[64:65]\n"
      "s_xor_b64 s[66:67], s[66:67], s[44:45]\n"
      "s_or_b64 vcc, vcc, s[66:67]\n"
      "s_xor_b64 s[68:69], s[68:69], s[44:45]\n"
      "s_or_b64 vcc, vcc, s[68:69]\n"
      "s_xor_b64 s[70:71], s[70:71], s[44:45]\n"
      "s_or_b64 vcc, vcc, s[70:71]\n"
      "s_xor_b64 s[72:73], s[72:73], s[44:45]\n"
      "s_or_b64 vcc, vcc, s[72:73]\n"
      "s_xor_b64 s[74:75], s[74:75], s[44:45]\n"
      "s_or_b64 vcc, vcc, s[74:75]\n"
      "s_xor_b64 s[76:77], s[76:77], s[44:45]\n"
      "s_or_b64 vcc, vcc, s[76:77]\n"
      "s_xor_b64 s[78:79], s[78:79], s[44:45]\n"
      "s_or_b64 vcc, vcc, s[78:79]\n"
      "s_cbranch_scc0 .Lq_fpe105\n"
      "v_and_b32 v4, 0xf0, v3\n"
      "s_movk_i32 s50, 5888\n"
      "s_mov_b32 s54, 262144\n"
      ".Lq_fp104:\n"
      "buffer_load_dwordx2 v[16:17], v4, s[32:35], s50 offen sc1\n"
      "s_sub_u32 s54, s54, 1\n"
      "s_cbranch_scc1 .Lq_fpe105\n"
      "s_waitcnt vmcnt(0)\n"
      "v_cmp_ne_u64 vcc, s[44:45], v[16:17]\n"
      "s_cbranch_vccz .Lq_fpe105\n"
      "s_sleep 8\n"
      "s_branch .Lq_fp104\n"
      ".Lq_fpe105:\n"
      "v_mov_b32 v2, 1\n"
      "ds_write_b32 v7, v2 offset:256\n"
      "s_setprio 0\n"
      "s_lshr_b32 s52, s37, 2\n"
      "s_mul_i32 s52, s52, 12845056\n"
      "s_lshl_b32 s50, s42, 7\n"
      "s_add_u32 s52, s52, s50\n"
      "s_add_u32 s52, s52, 7798784\n"
      "s_mov_b32 m0, 49152\n"
      "s_mov_b32 s53, 12\n"
      "v_and_b32 v1, 3, v0\n"
      "v_lshl_add_u32 v1, v1, 8, v6\n"
      "s_mov_b32 s54, 262144\n"
      ".Lq_sp4106:\n"
      "ds_read_b32 v2, v1\n"
      "s_sub_u32 s54, s54, 1\n"
      "s_cbranch_scc1 .Lq_s4e107\n"
      "s_waitcnt lgkmcnt(0)\n"
      "v_cmp_gt_u32 vcc, 28, v2\n"
      "s_cbranch_vccz .Lq_s4e107\n"
      "s_sleep 1\n"
      "s_branch .Lq_sp4106\n"
      ".Lq_s4e107:\n"
      "v_mov_b32 v5, 10\n"
      "s_mov_b32 s53, 0xfffffffe\n"
      ".Lq_vl108:\n"
      "v_max_i32 v2, 0, v5\n"
      "s_waitcnt vmcnt(15)\n"
      "buffer_load_dwordx4 v8, s[24:27], s52 offen offset:0 nt lds\n"
      "s_waitcnt vmcnt(15)\n"
      "buffer_load_dwordx4 v9, s[24:27], s52 offen offset:1024 nt lds\n"
      "s_waitcnt vmcnt(15)\n"
      "buffer_load_dwordx4 v10, s[24:27], s52 offen offset:2048 nt lds\n"
      "s_waitcnt vmcnt(15)\n"
      "buffer_load_dwordx4 v11, s[24:27], s52 offen offset:3072 nt lds\n"
      "s_add_u32 m0, m0, 0x1000\n"
      "s_add_u32 s52, s52, 2523136\n"
      "s_waitcnt vmcnt(15)\n"
      "buffer_load_dwordx4 v8, s[24:27], s52 offen offset:0 nt lds\n"
      "s_waitcnt vmcnt(15)\n"
      "buffer_load_dwordx4 v9, s[24:27], s52 offen offset:1024 nt lds\n"
      "s_waitcnt vmcnt(15)\n"
      "buffer_load_dwordx4 v10, s[24:27], s52 offen offset:2048 nt lds\n"
      "s_waitcnt vmcnt(15)\n"
      "ds_write_b32 v7, v2\n"
      "buffer_load_dwordx4 v11, s[24:27], s52 offen offset:3072 nt lds\n"
      "s_add_u32 m0, m0, 0x1000\n"
      "s_sub_u32 s52, s52, 2293760\n"
      "v_add_u32 v5, 2, v5\n"
      "s_add_u32 s53, s53, 1\n"
      "s_cbranch_scc0 .Lq_vl108\n"
      "v_and_b32 v1, 3, v0\n"
      "v_lshl_add_u32 v1, v1, 8, v6\n"
      "s_mov_b32 s54, 262144\n"
      ".Lq_sp4109:\n"
      "ds_read_b32 v2, v1\n"
      "s_sub_u32 s54, s54, 1\n"
      "s_cbranch_scc1 .Lq_s4e110\n"
      "s_waitcnt lgkmcnt(0)\n"
      "v_cmp_gt_u32 vcc, 65, v2\n"
      "s_cbranch_vccz .Lq_s4e110\n"
      "s_sleep 1\n"
      "s_branch .Lq_sp4109\n"
      ".Lq_s4e110:\n"
      "s_cmp_lt_u32 s39, 34\n"
      "s_cbranch_scc0 .Lq_mb0\n"
      "s_mov_b32 exec_hi, 0xffffff\n"
      "buffer_store_dword v12, v14, s[32:35], s61 offen sc1\n"
      "s_mov_b32 exec_hi, -1\n"
      ".Lq_mb0:\n"
      "v_mov_b32 v5, 10\n"
      "s_mov_b32 s53, 0xfffffff7\n"
      ".Lq_vl111:\n"
      "v_max_i32 v2, 12, v5\n"
      "s_waitcnt vmcnt(31)\n"
      "buffer_load_dwordx4 v8, s[24:27], s52 offen offset:0 nt lds\n"
      "s_waitcnt vmcnt(31)\n"
      "buffer_load_dwordx4 v9, s[24:27], s52 offen offset:1024 nt lds\n"
      "s_waitcnt vmcnt(31)\n"
      "buffer_load_dwordx4 v10, s[24:27], s52 offen offset:2048 nt lds\n"
      "s_waitcnt vmcnt(31)\n"
      "buffer_load_dwordx4 v11, s[24:27], s52 offen offset:3072 nt lds\n"
      "s_add_u32 m0, m0, 0x1000\n"
      "s_add_u32 s52, s52, 2523136\n"
      "s_waitcnt vmcnt(31)\n"
      "buffer_load_dwordx4 v8, s[24:27], s52 offen offset:0 nt lds\n"
      "s_waitcnt vmcnt(31)\n"
      "buffer_load_dwordx4 v9, s[24:27], s52 offen offset:1024 nt lds\n"
      "s_waitcnt vmcnt(31)\n"
      "buffer_load_dwordx4 v10, s[24:27], s52 offen offset:2048 nt lds\n"
      "s_waitcnt vmcnt(31)\n"
      "ds_write_b32 v7, v2\n"
      "buffer_load_dwordx4 v11, s[24:27], s52 offen offset:3072 nt lds\n"
      "s_add_u32 m0, m0, 0x1000\n"
      "s_sub_u32 s52, s52, 2293760\n"
      "v_add_u32 v5, 2, v5\n"
      "s_add_u32 s53, s53, 1\n"
      "s_cbranch_scc0 .Lq_vl111\n"
      ".Lq_vtail:\n"
      "v_max_i32 v2, 28, v2\n"
      "s_waitcnt vmcnt(24)\n"
      "ds_write_b32 v7, v2\n"
      "v_max_i32 v2, 30, v2\n"
      "s_waitcnt vmcnt(16)\n"
      "ds_write_b32 v7, v2\n"
      "v_max_i32 v2, 32, v2\n"
      "s_waitcnt vmcnt(8)\n"
      "ds_write_b32 v7, v2\n"
      "v_max_i32 v2, 34, v2\n"
      "s_waitcnt vmcnt(0)\n"
      "ds_write_b32 v7, v2\n"
      "s_lshr_b32 s41, s37, 2\n"
      "s_lshl_b32 s41, s41, 1\n"
      "s_cmp_ge_u32 s38, 14\n"
      "s_addc_u32 s41, s41, 0\n"
      "v_lshlrev_b32 v4, 7, v0\n"
      "s_lshl_b32 s52, s42, 2\n"
      "s_add_u32 s52, s52, s41\n"
      "s_mul_i32 s52, s52, 384\n"
      "s_add_u32 s53, s52, 3407872\n"
      "s_mov_b64 exec, 7\n"
      "s_mov_b32 s54, 262144\n"
      "s_sleep 10\n"
      "s_cmp_ge_u32 s38, 14\n"
      "s_cbranch_scc0 .Lq_hp\n"
      "s_sleep 14\n"
      ".Lq_hp:\n"
      "s_load_dwordx2 s[4:5], s[32:33], s53 glc\n"
      "s_load_dwordx2 s[6:7], s[32:33], s53 offset:0x80 glc\n"
      "s_load_dwordx2 s[8:9], s[32:33], s53 offset:0x100 glc\n"
      "s_sub_u32 s54, s54, 2048\n"
      "s_cbranch_scc1 .Lq_hpe\n"
      "s_waitcnt lgkmcnt(0)\n"
      "s_xor_b64 s[4:5], s[4:5], s[44:45]\n"
      "s_xor_b64 s[6:7], s[6:7], s[44:45]\n"
      "s_xor_b64 s[8:9], s[8:9], s[44:45]\n"
      "s_or_b64 s[4:5], s[4:5], s[6:7]\n"
      "s_or_b64 s[4:5], s[4:5], s[8:9]\n"
      "s_cbranch_scc0 .Lq_hpe\n"
      "s_sleep 2\n"
      "s_branch .Lq_hp\n"
      ".Lq_hpe:\n"
      "s_sleep 2\n"
      "v_mov_b32 v2, 200\n"
      "ds_write_b32 v7, v2\n"
      "s_endpgm\n"
      ".Lq_spare:\n"
      "s_mov_b32 s51, 1376256\n"
      "s_bitcmp1_b32 s38, 1\n"
      "s_cselect_b32 s50, 8028160, s51\n"
      "s_cselect_b32 s53, 10, 11\n"
      "s_cselect_b32 s51, 11, 17\n"
      "s_add_u32 s52, s52, s50\n"
      "s_lshl_b32 s51, s51, 5\n"
      "v_lshrrev_b32 v1, 5, v0\n"
      "v_and_b32 v2, 31, v0\n"
      "v_mad_u32_u24 v1, v1, s51, v2\n"
      "v_mul_u32_u24 v1, 7168, v1\n"
      "s_cmp_eq_u32 s53, 0\n"
      "s_cbranch_scc1 .Lq_sp2\n"
      ".Lq_sp1:\n"
      "buffer_load_dword v20, v1, s[24:27], s52 offen\n"
      "s_waitcnt vmcnt(3)\n"
      "s_add_u32 s52, s52, 229376\n"
      "s_sub_u32 s53, s53, 1\n"
      "s_cmp_lg_u32 s53, 0\n"
      "s_cbranch_scc1 .Lq_sp1\n"
      ".Lq_sp2:\n"
      "s_endpgm\n"
      ".Lq_retire:\n"
      "ds_read_b32 v12, v6 offset:1536\n"
      "ds_read_b32 v13, v6 offset:1792\n"
      "s_sleep 80\n"
      "s_waitcnt lgkmcnt(0)\n"
      "v_readfirstlane_b32 s44, v12\n"
      "v_readfirstlane_b32 s45, v13\n"
      "s_movk_i32 s52, 4096\n"
      ".Lq_ret:\n"
      "v_mov_b32 v4, v3\n"
      "s_mov_b32 s50, s52\n"
      "s_mov_b32 s54, 262144\n"
      ".Lq_fp112:\n"
      "buffer_load_dwordx2 v[16:17], v4, s[32:35], s50 offen sc1\n"
      "s_sub_u32 s54, s54, 1\n"
      "s_cbranch_scc1 .Lq_fpe113\n"
      "s_waitcnt vmcnt(0)\n"
      "v_cmp_ne_u64 vcc, s[44:45], v[16:17]\n"
      "s_cbranch_vccz .Lq_fpe113\n"
      "s_sleep 8\n"
      "s_branch .Lq_fp112\n"
      ".Lq_fpe113:\n"
      "s_add_u32 s52, s52, 0x400\n"
      "s_cmp_lt_u32 s52, 8192\n"
      "s_cbranch_scc1 .Lq_ret\n"
      "s_mov_b64 exec, 1\n"
      "buffer_store_dwordx2 v[12:13], v0, s[32:35], 0 offen sc1\n"
      "s_mov_b64 exec, -1\n"
      "s_endpgm\n"
      ".Lq_last:\n"
      :
      : "{s[0:1]}"(__builtin_amdgcn_kernarg_segment_ptr()), "{s2}"(__builtin_amdgcn_workgroup_id_x()),
        "{v0}"(__builtin_amdgcn_workitem_id_x()), "{s3}"((unsigned)(uintptr_t)(__attribute__((address_space(3))) unsigned char*)smem)
      : "v1", "v2", "v3", "v4", "v5", "v6", "v7", "v8", "v9", "v10", "v11", "v12", "v13", "v14", "v15", "v16", "v17", "v18", "v19", "v20", "v21", "v22", "v23", "v24", "v25", "v26", "v27", "v28", "v29", "v30", "v31", "v32", "v33", "v34", "v35", "v36", "v37", "v38", "v39", "v40", "v41", "v42", "v43", "v44", "v45", "v46", "v47", "v48", "v49", "v50", "v51", "v52", "v53", "v54", "v55", "v56", "v57", "v58", "v59", "v60", "v61", "v62", "v63", "v64", "v65", "v66", "v67", "v68", "v69", "v70", "v71", "v72", "v73", "v74", "v75", "v76", "v77", "v78", "v79", "v80", "v81", "v82", "v83", "v84", "v85", "v86", "v87", "v88", "v89", "v90", "v91", "v92", "v93", "v94", "v95", "v96", "v97", "v98", "v99", "v100", "v101", "v102", "v103", "v104", "v105", "v106", "v107", "v108", "v109", "v110", "v111", "v112", "v113", "v114", "v115", "v116", "v117", "v118", "v119", "v120", "v121", "v122", "v123", "v124", "v125", "v126", "v127", "v128", "v129", "v130", "v131", "v132", "v133", "v134", "v135", "v136", "v137", "v138", "v139", "v140", "v141", "v142", "v143", "v144", "v145", "v146", "v147", "v148", "v149", "v150", "v151", "v152", "v153", "v154", "v155", "v156", "v157", "v158", "v159", "v160", "v161", "v162", "v163", "v164", "v165", "v166", "v167", "v168", "v169", "v170", "v171", "v172", "v173", "v174", "v175", "v176", "v177", "v178", "v179", "v180", "v181", "v182", "v183", "v184", "v185", "v186", "v187", "v188", "v189", "v190", "v191", "v192", "v193", "v194", "v195", "v196", "v197", "v198", "v199", "v200", "v201", "v202", "v203", "v204", "v205", "v206", "v207", "v208", "v209", "v210", "v211", "v212", "v213", "v214", "v215", "v216", "v217", "v218", "v219", "v220", "v221", "v222", "v223", "v224", "v225", "v226", "v227", "v228", "v229", "v230", "v231", "v232", "v233", "v234", "v235", "s4", "s5", "s6", "s7", "s8", "s9", "s10", "s11", "s12", "s13", "s14", "s15", "s16", "s17", "s18", "s19", "s20", "s21", "s22", "s23", "s24", "s25", "s26", "s27", "s28", "s29", "s30", "s31", "s33", "s34", "s35", "s36", "s37", "s38", "s39", "s40", "s41", "s42", "s43", "s44", "s45", "s46", "s47", "s48", "s49", "s50", "s51", "s52", "s53", "s54", "s55", "s56", "s57", "s58", "s59", "s60", "s61", "s62", "s63", "s64", "s65", "s66", "s67", "s68", "s69", "s70", "s71", "s72", "s73", "s74", "s75", "s76", "s77", "s78", "s79", "s80", "s81", "s82", "s83", "s84", "s85", "s86", "s87", "s88", "s89", "s90", "s91", "s92", "s93", "s94", "s95", "s96", "s97", "vcc", "memory");
}

extern "C" void kernel_launch(void* const* d_in, const int* in_sizes, int n_in, void* d_out, int out_size, void* d_ws, size_t ws_size,
                              hipStream_t stream) {
  const unsigned f = 0x00020000u;
  quip_kernel<<<256, 320, 0, stream>>>(d_in[0], 64u * 1024u * 2u, f, d_in[1], 1024u * 3584u * 4u, f, d_in[2], 32u * 3584u * 2u, f,
                                       d_in[3], 32u * 3584u * 2u, f, d_in[4], 1024u * 1024u * 2u, f, d_in[5], 3584u * 3584u * 2u, f,
                                       d_out, 64u * 3584u * 2u, f, d_ws, 3493888u, f);
}
```
